```python
import torch

def _softmax(x, axis=-1):
    return torch.softmax(x, dim=axis)
def _gelu(x):
    return torch.nn.functional.gelu(x, approximate="tanh")
def _silu(x):
    return torch.nn.functional.silu(x)
def _sigmoid(x):
    return torch.sigmoid(x)
def _layernorm(x, axis=-1, eps=1e-5):
    m = x.mean(dim=axis, keepdim=True)
    v = x.var(dim=axis, keepdim=True, unbiased=False)
    return (x - m) / torch.sqrt(v + eps)
def _scatter_add(tgt, idx, src, axis=0):
    return tgt.clone().index_add_(axis, idx, src)
def _segment_sum(src, seg, n):
    out = torch.zeros((n,) + src.shape[1:], dtype=src.dtype)
    return out.index_add_(0, seg, src)
def _pad(x, pw):
    flat = tuple(v for pair in reversed(pw) for v in pair)
    return torch.nn.functional.pad(x, flat)

kernel_name = 'sequential_preLN__ssm_conv_s4d__mixer_s_mlp__prompt'

def setup_inputs():

    _w = (lambda a: (lambda t: t.to(torch.bfloat16) if t.is_floating_point() else t)(torch.as_tensor(a)))
    def _A_setup():
        Bsz, L, D, N = 1, 2048, 640, 128
        return {
            "u": _w((lambda *s: torch.randn(*s, dtype=torch.bfloat16) * 0.05)(Bsz, L, D)),
            "log_A_real": _w((lambda *s: torch.randn(*s, dtype=torch.bfloat16) * 6.4)(D, N) * 0.5),
            "A_imag": _w((lambda *s: torch.randn(*s, dtype=torch.bfloat16) * 0.05)(D, N) * 3.14),
            "C_real": _w((lambda *s: torch.randn(*s, dtype=torch.bfloat16) * 0.05)(D, N) / N ** 0.5),
            "C_imag": _w((lambda *s: torch.randn(*s, dtype=torch.bfloat16) * 0.05)(D, N) / N ** 0.5),
            "log_dt": _w((lambda *s: torch.randn(*s, dtype=torch.bfloat16) * 0.05)(D) * 0.5 - 4.0),
            "D_skip": _w((lambda *s: torch.randn(*s, dtype=torch.bfloat16) * 0.05)(D)),
        }
    _A_in = _A_setup()

    def _B_setup():
        B, S, D, F = 1, 2048, 640, 1728
        return {
            "x":  _w((lambda *s: torch.randn(*s, dtype=torch.bfloat16) * 0.05)(B, S, D)),
            "w1": _w((lambda *s: torch.randn(*s, dtype=torch.bfloat16) * 6.4)(D, D) * (D ** -0.5)),
            "w2": _w((lambda *s: torch.randn(*s, dtype=torch.bfloat16) * 6.4)(D, D) * (D ** -0.5)),
            "w3": _w((lambda *s: torch.randn(*s, dtype=torch.bfloat16) * 6.4)(D, F) * (D ** -0.5)),
            "w4": _w((lambda *s: torch.randn(*s, dtype=torch.bfloat16) * 6.4)(F, D) * (F ** -0.5)),
        }
    _B_in = _B_setup()

    return {
        "A_u": _A_in["u"],
        "A_log_A_real": _A_in["log_A_real"],
        "A_A_imag": _A_in["A_imag"],
        "A_C_real": _A_in["C_real"],
        "A_C_imag": _A_in["C_imag"],
        "A_log_dt": _A_in["log_dt"],
        "A_D_skip": _A_in["D_skip"],
        "B_x": _B_in["x"],
        "B_w1": _B_in["w1"],
        "B_w2": _B_in["w2"],
        "B_w3": _B_in["w3"],
        "B_w4": _B_in["w4"],
    }

def reference(A_u, A_log_A_real, A_A_imag, A_C_real, A_C_imag, A_log_dt, A_D_skip, B_x, B_w1, B_w2, B_w3, B_w4):
    def _A_ref(u, log_A_real, A_imag, C_real, C_imag, log_dt, D_skip):
        L = u.shape[1]
        dt = torch.exp(log_dt)
        A = -torch.exp(log_A_real) + 1j * A_imag
        C = (C_real + 1j * C_imag) * dt[:, None]
        pos = (lambda x, dt: x.to(dt))(torch.arange(L), log_dt.dtype)
        P = torch.exp((A * dt[:, None])[:, :, None] * pos[None, None, :])
        K = 2.0 * torch.einsum("dn,dnl->dl", C, P).real
        L2 = 2 * L
        u_t = (lambda x, *d: x.permute(*d))(u, 0, 2, 1)
        Kf = (lambda x, *a, **k: torch.fft.rfft(x.float(), *a, **k))(K, L2)
        Uf = (lambda x, *a, **k: torch.fft.rfft(x.float(), *a, **k))(u_t, L2)
        y = (lambda x, *a, **k: torch.fft.irfft(x, *a, **k).to(torch.bfloat16))(Uf * Kf[None, :, :], L2)[:, :, :L]
        y = (lambda x, *d: x.permute(*d))(y, 0, 2, 1)
        return y + u * D_skip[None, None, :]

    def _B_ref(x, w1, w2, w3, w4):
        B, S, D = x.shape
        h = _gelu(_layernorm(x, axis=-1) @ w1)
        c = D // 4
        g0 = _pad(h[:, :, 0*c:1*c], ((0, 0), (1, 0), (0, 0)))[:, :S, :]
        g1 = _pad(h[:, :, 1*c:2*c], ((0, 0), (0, 1), (0, 0)))[:, 1:, :]
        g2 = _pad(h[:, :, 2*c:3*c], ((0, 0), (2, 0), (0, 0)))[:, :S, :]
        g3 = _pad(h[:, :, 3*c:   ], ((0, 0), (0, 2), (0, 0)))[:, 2:, :]
        hs = torch.concatenate([g0, g1, g2, g3], axis=-1) @ w2
        x1 = x + hs
        h2 = _gelu(_layernorm(x1, axis=-1) @ w3) @ w4
        return x1 + h2

    x = A_u
    h = x + _A_ref(_layernorm(x, axis=-1), A_log_A_real, A_A_imag, A_C_real, A_C_imag, A_log_dt, A_D_skip)
    h2 = h + _B_ref(_layernorm(h, axis=-1), B_w1, B_w2, B_w3, B_w4)
    return h2
```

```cpp
#include <hip/hip_runtime.h>
#include <hip/hip_bf16.h>
#include <stdint.h>

typedef unsigned short u16;
typedef __attribute__((ext_vector_type(4))) unsigned u32x4;
typedef __attribute__((ext_vector_type(2))) unsigned u32x2;
typedef __attribute__((ext_vector_type(4))) float f32x4;
typedef __attribute__((ext_vector_type(2))) float f32x2;
typedef __attribute__((ext_vector_type(8))) __bf16 bf16x8;
typedef __attribute__((ext_vector_type(2))) __bf16 bf16x2;
typedef __attribute__((ext_vector_type(8))) _Float16 f16x8;
typedef __attribute__((ext_vector_type(2))) _Float16 f16x2;
typedef __attribute__((ext_vector_type(4))) short s16x4;
#define LDSP __attribute__((address_space(3)))
#define DEV __device__ __forceinline__

constexpr int SL = 2048, DM = 640, NS = 128, FF = 1728;
constexpr int KRW = 2064;

constexpr size_t al4k(size_t x) { return (x + 4095) & ~(size_t)4095; }
constexpr size_t ACT = (size_t)SL * DM * 2;
constexpr size_t O_UT = 0;
constexpr size_t O_XT = O_UT + al4k(ACT);
constexpr size_t O_KR = O_XT + al4k(ACT);
constexpr size_t O_KS = O_KR + al4k((size_t)DM * KRW * 2);
constexpr size_t O_HT = O_KS + 4096;
constexpr size_t O_W1 = O_HT + al4k(ACT);
constexpr size_t O_W2 = O_W1 + al4k((size_t)DM * DM * 2);
constexpr size_t O_W3 = O_W2 + al4k((size_t)DM * DM * 2);
constexpr size_t O_W4 = O_W3 + al4k((size_t)DM * FF * 2);
constexpr size_t O_CS1 = O_W4 + al4k((size_t)DM * FF * 2);
constexpr size_t O_CS3 = O_CS1 + 4096;
constexpr size_t O_HB = O_CS3 + 8192;
constexpr size_t O_ZD = O_HB + al4k(ACT);
constexpr size_t O_HD = O_ZD + al4k(ACT);
constexpr size_t O_X1 = O_HD + al4k(ACT);
constexpr size_t O_A3 = O_X1 + al4k(ACT);
constexpr size_t O_END = O_A3 + al4k((size_t)SL * FF * 2);

DEV float bf2f(u16 v) { return __uint_as_float((unsigned)v << 16); }
DEV float blo(unsigned v) { return __uint_as_float(v << 16); }
DEV float bhi(unsigned v) { return __uint_as_float(v & 0xffff0000u); }
DEV unsigned pkbf(float a, float b) {
  f32x2 f = {a, b};
  return __builtin_bit_cast(unsigned, __builtin_convertvector(f, bf16x2));
}
DEV unsigned pkh(float a, float b) {
  f32x2 f = {a, b};
  return __builtin_bit_cast(unsigned, __builtin_convertvector(f, f16x2));
}
DEV float rbf(float a) { return blo(pkbf(a, a)); }
DEV float gelu_f(float x) {
  const float C1 = -2.0f * 0.7978845608028654f * 1.4426950408889634f;
  const float C2 = C1 * 0.044715f;
  float t = x * fmaf(x * x, C2, C1);
  float e = __builtin_amdgcn_exp2f(t);
  return x * __builtin_amdgcn_rcpf(1.0f + e);
}
DEV u32x2 tr_read(const char* lds_base, unsigned off) {
  s16x4 v = __builtin_amdgcn_ds_read_tr16_b64_v4i16((LDSP s16x4*)(lds_base + off));
  return __builtin_bit_cast(u32x2, v);
}
DEV f32x4 mfma_bf(u32x4 a, u32x4 b, f32x4 c) {
  return __builtin_amdgcn_mfma_f32_16x16x32_bf16(__builtin_bit_cast(bf16x8, a), __builtin_bit_cast(bf16x8, b), c, 0, 0, 0);
}
DEV f32x4 mfma_h(u32x4 a, u32x4 b, f32x4 c) {
  return __builtin_amdgcn_mfma_f32_16x16x32_f16(__builtin_bit_cast(f16x8, a), __builtin_bit_cast(f16x8, b), c, 0, 0, 0);
}
DEV float dot2bf(unsigned a, unsigned b, float c) {
  return __builtin_amdgcn_fdot2_f32_bf16(__builtin_bit_cast(bf16x2, a), __builtin_bit_cast(bf16x2, b), c, false);
}
DEV unsigned lds_addr(const void* p) { return (unsigned)(size_t)(LDSP const char*)p; }

#define DMA16(lds, g, v, OFF)                                                                          \
  do {                                                                                                 \
    unsigned keep_;                                                                                    \
    asm volatile("s_mov_b32 %0, m0\n\ts_mov_b32 m0, %1\n\ts_nop 0\n\t"                                 \
                 "global_load_lds_dwordx4 %2, %3 offset:%4\n\ts_mov_b32 m0, %0"                        \
                 : "=&s"(keep_) : "s"(lds), "v"(v), "s"(g), "i"(OFF) : "memory");                      \
  } while (0)
#define GLD16(dst, g, v, OFF) \
  asm volatile("global_load_dwordx4 %0, %1, %2 offset:%3" : "=v"(dst) : "v"(v), "s"(g), "i"(OFF) : "memory")

DEV void ln_scalars(float mean, float var, float& mb, float& sb) {
  mb = rbf(mean);
  sb = rbf(__builtin_sqrtf(rbf(rbf(var) + 1e-5f)));
}

constexpr int NB_LN = 128, NB_KG = 640, NB_PK = 185, NB_CS = 37;

struct c32 { float x, y; };
DEV c32 cmul(c32 a, c32 b) { return {fmaf(a.x, b.x, -a.y * b.y), fmaf(a.x, b.y, a.y * b.x)}; }

DEV void role_ln(int b, const u16* __restrict__ X, char* __restrict__ ws, char* sm) {
  const int tid = threadIdx.x;
  const int tile = (b & 7) * 16 + (b >> 3);
  const int t0 = tile * 16, r = tid >> 4, sub = tid & 15;
  const u32x4* src = (const u32x4*)(X + (size_t)(t0 + r) * DM);
  u32x4 v[5];
#pragma unroll
  for (int j = 0; j < 5; ++j) v[j] = src[j * 16 + sub];
  const int fr = ((r & 3) | ((r >> 3) << 2)) << 1;
  float s = 0.f;
#pragma unroll
  for (int j = 0; j < 5; ++j) {
    *(u32x4*)(sm + r * 1280 + (((j * 16 + sub) ^ fr) << 4)) = v[j];
#pragma unroll
    for (int e = 0; e < 4; ++e) s += blo(v[j][e]) + bhi(v[j][e]);
  }
#pragma unroll
  for (int o = 1; o < 16; o <<= 1) s += __shfl_xor(s, o);
  const float mean = s * (1.0f / DM);
  float q = 0.f;
#pragma unroll
  for (int j = 0; j < 5; ++j)
#pragma unroll
    for (int e = 0; e < 4; ++e) {
      float a = blo(v[j][e]) - mean, c = bhi(v[j][e]) - mean;
      q = fmaf(a, a, q);
      q = fmaf(c, c, q);
    }
#pragma unroll
  for (int o = 1; o < 16; o <<= 1) q += __shfl_xor(q, o);
  float mb, sb;
  ln_scalars(mean, q * (1.0f / DM), mb, sb);
  const float is = 1.0f / sb;
#pragma unroll
  for (int j = 0; j < 5; ++j) {
    u32x4 o;
#pragma unroll
    for (int e = 0; e < 4; ++e) {
      unsigned dd = pkbf(blo(v[j][e]) - mb, bhi(v[j][e]) - mb);
      unsigned uu = pkbf(blo(dd) * is, bhi(dd) * is);
      o[e] = pkh(blo(uu), bhi(uu));
    }
    *(u32x4*)(sm + 20480 + r * 1280 + (((j * 16 + sub) ^ fr) << 4)) = o;
  }
  __syncthreads();
  const int lane = tid & 63, w = tid >> 6, g = lane >> 4, i = lane & 15, qq = i >> 2, p = i & 3;
  const int oct = g & 1, cb = g >> 1;
  const int r0 = oct * 8 + qq, r1 = r0 + 4;
  const int f0 = ((r0 & 3) | ((r0 >> 3) << 2)) << 1, f1 = f0;
#pragma unroll
  for (int it = 0; it < 5; ++it) {
    const int c0 = (it * 4 + w) * 32 + cb * 16;
    const int ch = (c0 >> 3) + (p >> 1);
    const unsigned a0 = r0 * 1280 + ((ch ^ f0) << 4) + 8 * (p & 1);
    const unsigned a1 = r1 * 1280 + ((ch ^ f1) << 4) + 8 * (p & 1);
    u32x2 x0 = tr_read(sm, a0), x1 = tr_read(sm, a1);
    u32x2 u0 = tr_read(sm, 20480 + a0), u1 = tr_read(sm, 20480 + a1);
    const size_t go = ((size_t)(c0 + i) * SL + t0 + oct * 8) * 2;
    *(u32x4*)(ws + O_XT + go) = u32x4{x0[0], x0[1], x1[0], x1[1]};
    *(u32x4*)(ws + O_UT + go) = u32x4{u0[0], u0[1], u1[0], u1[1]};
  }
}

constexpr int FE_ROW = 528;
constexpr int FE_T = 64 * FE_ROW;

DEV void role_kgen(int d, const u16* __restrict__ lAr, const u16* __restrict__ Aim, const u16* __restrict__ Cre,
                   const u16* __restrict__ Cim, const u16* __restrict__ ldt, char* __restrict__ ws, char* sm) {
  const int tid = threadIdx.x, n = tid & 127, half = tid >> 7, lane = tid & 63;
  const u16 cr_b = Cre[d * NS + n], ci_b = Cim[d * NS + n];
  const u16 cr_o = Cre[d * NS + (n ^ 64)], ci_o = Cim[d * NS + (n ^ 64)];
  const float dt = rbf(expf(bf2f(ldt[d])));
  const float are = rbf(expf(bf2f(lAr[d * NS + n])));
  const float ar = -are * dt;
  const float ai = bf2f(Aim[d * NS + n]) * dt;
  unsigned mx = max(max(cr_b & 0x7fff, ci_b & 0x7fff), max(cr_o & 0x7fff, ci_o & 0x7fff));
#pragma unroll
  for (int o = 1; o < 64; o <<= 1) mx = max(mx, (unsigned)__shfl_xor((int)mx, o));
  unsigned ef = mx >> 7;
  ef = min(max(ef, 10u), 254u);
  const float sF = __uint_as_float((264u - ef) << 23);
  const float ar2 = ar * 1.4426950408889634f;
  const float I2PH = 0.15915494309189535f;
  const float I2PL = (float)(0.15915494309189533577 - (double)0.15915494309189535f);
  const float chi = ai * I2PH;
  const float clo = fmaf(ai, I2PH, -chi) + ai * I2PL;
  auto anchor = [&](float p) -> c32 {
    float m = __builtin_amdgcn_exp2f(p * ar2);
    float rv = fmaf(p, clo, __builtin_amdgcn_fractf(p * chi));
    return {m * __builtin_amdgcn_cosf(rv), m * __builtin_amdgcn_sinf(rv)};
  };
  unsigned* tab = (unsigned*)(sm + n * 4);
  auto putF = [&](int row, c32 v) { tab[row * (FE_ROW / 4)] = pkh(v.x, -v.y); };
  auto putE = [&](int row, c32 v) { tab[(48 + row) * (FE_ROW / 4)] = pkh(v.x, v.y); };
  const c32 C0 = {bf2f(cr_b) * sF, bf2f(ci_b) * sF};
  if (half == 0) {
    c32 e[16];
    e[0] = {1024.f, 0.f};
#pragma unroll
    for (int k = 0; k < 4; ++k) {
      c32 wk = anchor((float)(8 << k));
#pragma unroll
      for (int j = 0; j < (1 << k); ++j) e[(1 << k) + j] = cmul(e[j], wk);
    }
#pragma unroll
    for (int j = 0; j < 16; ++j) putE(j, e[j]);
    c32 h[8];
    h[0] = C0;
#pragma unroll
    for (int k = 0; k < 3; ++k) {
      c32 wk = anchor((float)(1 << k));
#pragma unroll
      for (int j = 0; j < (1 << k); ++j) h[(1 << k) + j] = cmul(h[j], wk);
    }
    c32 w128 = anchor(128.f);
#pragma unroll
    for (int j = 0; j < 8; ++j) {
      putF(j, h[j]);
      putF(8 + j, cmul(h[j], w128));
    }
  } else {
    c32 gq[17];
    gq[0] = C0;
#pragma unroll
    for (int k = 0; k < 4; ++k) {
      c32 wk = anchor((float)(128 << k));
#pragma unroll
      for (int j = 0; j < (1 << k); ++j) gq[(1 << k) + j] = cmul(gq[j], wk);
    }
    gq[16] = cmul(C0, anchor(2048.f));
    c32 w2 = anchor(2.f), w4 = anchor(4.f);
#pragma unroll
    for (int I = 2; I < 4; ++I) {
      c32 a2 = cmul(gq[I], w2);
      putF(16 + (I - 2) * 4 + 0, gq[I]);
      putF(16 + (I - 2) * 4 + 1, a2);
      putF(16 + (I - 2) * 4 + 2, cmul(gq[I], w4));
      putF(16 + (I - 2) * 4 + 3, cmul(a2, w4));
    }
#pragma unroll
    for (int I = 4; I < 8; ++I) {
      putF(24 + (I - 4) * 2, gq[I]);
      putF(24 + (I - 4) * 2 + 1, cmul(gq[I], w4));
    }
#pragma unroll
    for (int I = 8; I < 17; ++I) putF(32 + (I - 8), gq[I]);
  }
  __syncthreads();
  const int w = __builtin_amdgcn_readfirstlane(tid >> 6);
  _Float16* T = (_Float16*)(sm + FE_T);
  if (w < 3) {
    const int r = lane & 15, g = lane >> 4;
    f32x4 acc = {0.f, 0.f, 0.f, 0.f};
    const char* pa = sm + (w * 16 + r) * FE_ROW + g * 16;
    const char* pb = sm + (48 + r) * FE_ROW + g * 16;
#pragma unroll
    for (int ks = 0; ks < 8; ++ks)
      acc = mfma_h(*(const u32x4*)(pa + ks * 64), *(const u32x4*)(pb + ks * 64), acc);
#pragma unroll
    for (int e = 0; e < 4; ++e) {
      const int rr = g * 4 + e;
      int hi;
      if (w == 0) hi = 128 * (rr >> 3) + (rr & 7);
      else if (w == 1) hi = rr < 8 ? 256 + 128 * (rr >> 2) + 2 * (rr & 3) : 512 + 128 * ((rr - 8) >> 1) + 4 * (rr & 1);
      else hi = 1024 + 128 * rr;
      if (w < 2 || rr <= 8) T[hi + 8 * r] = (_Float16)(acc[e] * (1.0f / 8192.0f));
    }
  }
  if (tid == 0) ((float*)(ws + O_KS))[d] = dt * __uint_as_float((ef - 6u) << 23);
  __syncthreads();
  u32x4 o;
#pragma unroll
  for (int e2 = 0; e2 < 4; ++e2) {
    unsigned hv[2];
#pragma unroll
    for (int s = 0; s < 2; ++s) {
      const int l = 2047 - (tid * 8 + e2 * 2 + s);
      unsigned fb = __float_as_uint((float)l);
      fb = (fb + 0x7fffu + ((fb >> 16) & 1u)) & 0xffff0000u;
      const int p = (int)__uint_as_float(fb);
      hv[s] = ((const u16*)T)[p];
    }
    o[e2] = hv[0] | (hv[1] << 16);
  }
  char* krow = ws + O_KR + (size_t)d * KRW * 2;
  *(u32x4*)(krow + tid * 16) = o;
  if (tid < 2) *(u32x4*)(krow + 4096 + tid * 16) = u32x4{0, 0, 0, 0};
}

DEV int swz128(int row) { return (((row >> 1) & 1) | (((row >> 3) & 1) << 1)) << 1; }

DEV void role_pack(int b, const u16* __restrict__ w1, const u16* __restrict__ w2, const u16* __restrict__ w3,
                   const u16* __restrict__ w4, char* __restrict__ ws, char* sm) {
  const int tid = threadIdx.x, lane = tid & 63, w = tid >> 6;
  char* my = sm + w * 8192;
  const u16* src[2];
  char* dst[2];
  int ldn[2], kst[2];
  u32x4 v[2][4];
#pragma unroll
  for (int s = 0; s < 2; ++s) {
    int un = (b * 4 + w) * 2 + s;
    const u16* W; char* P; int N, KS, ns;
    if (un < 200) { W = w1; P = ws + O_W1; N = DM; KS = 20; ns = 10; }
    else if (un < 400) { un -= 200; W = w2; P = ws + O_W2; N = DM; KS = 20; ns = 10; }
    else if (un < 940) { un -= 400; W = w3; P = ws + O_W3; N = FF; KS = 20; ns = 27; }
    else { un -= 940; W = w4; P = ws + O_W4; N = DM; KS = 54; ns = 10; }
    const int ks = un / ns, st = un - ks * ns;
    src[s] = W + (size_t)(ks * 32) * N + st * 64;
    dst[s] = P + ((size_t)(st * 4) * KS + ks) * 1024;
    ldn[s] = N; kst[s] = KS;
#pragma unroll
    for (int j = 0; j < 4; ++j)
      v[s][j] = *(const u32x4*)(src[s] + (size_t)(j * 8 + (lane >> 3)) * N + (lane & 7) * 8);
  }
#pragma unroll
  for (int s = 0; s < 2; ++s)
#pragma unroll
    for (int j = 0; j < 4; ++j) {
      const int row = j * 8 + (lane >> 3);
      *(u32x4*)(my + s * 4096 + row * 128 + (((lane & 7) ^ swz128(row)) << 4)) = v[s][j];
    }
  __syncthreads();
  const int g = lane >> 4, i = lane & 15, qq = i >> 2, p = i & 3;
#pragma unroll
  for (int s = 0; s < 2; ++s)
#pragma unroll
    for (int nt = 0; nt < 4; ++nt) {
      const int r0 = 8 * g + qq, r1 = r0 + 4;
      u32x2 a = tr_read(my, s * 4096 + r0 * 128 + (((2 * nt + (p >> 1)) ^ swz128(r0)) << 4) + 8 * (p & 1));
      u32x2 c = tr_read(my, s * 4096 + r1 * 128 + (((2 * nt + (p >> 1)) ^ swz128(r1)) << 4) + 8 * (p & 1));
      *(u32x4*)(dst[s] + (size_t)nt * kst[s] * 1024 + lane * 16) = u32x4{a[0], a[1], c[0], c[1]};
    }
  (void)ldn;
}

DEV void role_colsum(int b, const u16* __restrict__ w1, const u16* __restrict__ w3, char* __restrict__ ws, char* sm) {
  const int tid = threadIdx.x;
  const u16* W; float* out; int N;
  if (b < 10) { W = w1 + b * 64; out = (float*)(ws + O_CS1) + b * 64; N = DM; }
  else { W = w3 + (b - 10) * 64; out = (float*)(ws + O_CS3) + (b - 10) * 64; N = FF; }
  const int r = tid >> 3, c = tid & 7;
  u32x4 v[20];
#pragma unroll
  for (int j = 0; j < 20; ++j) v[j] = *(const u32x4*)(W + (size_t)(j * 32 + r) * N + c * 8);
  float a[8];
#pragma unroll
  for (int e = 0; e < 8; ++e) a[e] = 0.f;
#pragma unroll
  for (int j = 0; j < 20; ++j)
#pragma unroll
    for (int e = 0; e < 4; ++e) { a[2 * e] += blo(v[j][e]); a[2 * e + 1] += bhi(v[j][e]); }
  float* red = (float*)sm;
#pragma unroll
  for (int e = 0; e < 8; ++e) red[r * 65 + c * 8 + e] = a[e];
  __syncthreads();
  if (tid < 64) {
    float s = 0.f;
#pragma unroll
    for (int j = 0; j < 32; ++j) s += red[j * 65 + tid];
    out[tid] = s;
  }
}

__global__ __launch_bounds__(256, 4) void k_prep(const u16* __restrict__ X, const u16* __restrict__ lAr,
                                                 const u16* __restrict__ Aim, const u16* __restrict__ Cre,
                                                 const u16* __restrict__ Cim, const u16* __restrict__ ldt,
                                                 const u16* __restrict__ w1, const u16* __restrict__ w2,
                                                 const u16* __restrict__ w3, const u16* __restrict__ w4,
                                                 char* __restrict__ ws) {
  __shared__ __attribute__((aligned(16))) char sm[40960];
  int b = blockIdx.x;
  if (b < NB_LN) { role_ln(b, X, ws, sm); return; }
  b -= NB_LN;
  if (b < NB_CS) { role_colsum(b, w1, w3, ws, sm); return; }
  b -= NB_CS;
  if (b < NB_PK) { role_pack(b, w1, w2, w3, w4, ws, sm); return; }
  b -= NB_PK;
  role_kgen(b, lAr, Aim, Cre, Cim, ldt, ws, sm);
}

constexpr int KC_STR = 4160, KC_U = 8 * KC_STR;

__global__ __launch_bounds__(256, 3) void k_conv(char* __restrict__ ws, const u16* __restrict__ Dsk) {
  __shared__ __attribute__((aligned(16))) char sm[KC_U + 4608];
  const int d = blockIdx.x, tid = threadIdx.x, lane = tid & 63;
  const int w = __builtin_amdgcn_readfirstlane(tid >> 6);
  const int a = lane & 15, g = lane >> 4;
  const char* krow = ws + O_KR + (size_t)d * KRW * 2;
  const u32x4 k0 = *(const u32x4*)(krow + tid * 16);
  const u32x4 k1 = *(const u32x4*)(krow + tid * 16 + 16);
  const u32x4 uu = *(const u32x4*)(ws + O_UT + (size_t)d * SL * 2 + tid * 16);
  const int tok0 = 512 * w + 16 * a + 4 * g;
  const u32x2 xa = *(const u32x2*)(ws + O_XT + ((size_t)d * SL + tok0) * 2);
  const u32x2 xb = *(const u32x2*)(ws + O_XT + ((size_t)d * SL + tok0 + 256) * 2);
  const float kinv = ((const float*)(ws + O_KS))[d];
  const float dsk = bf2f(Dsk[d]);
  {
    unsigned wv[8] = {k0[0], k0[1], k0[2], k0[3], k1[0], k1[1], k1[2], k1[3]};
    unsigned al[7];
#pragma unroll
    for (int i = 0; i < 7; ++i) al[i] = __builtin_amdgcn_alignbit(wv[i + 1], wv[i], 16);
#pragma unroll
    for (int c = 0; c < 4; ++c) {
      *(u32x4*)(sm + (2 * c) * KC_STR + tid * 16) = u32x4{wv[c], wv[c + 1], wv[c + 2], wv[c + 3]};
      *(u32x4*)(sm + (2 * c + 1) * KC_STR + tid * 16) = u32x4{al[c], al[c + 1], al[c + 2], al[c + 3]};
    }
    if (tid < 8) *(u32x4*)(sm + tid * KC_STR + 4096) = u32x4{0, 0, 0, 0};
    *(u32x4*)(sm + KC_U + 512 + tid * 16) = uu;
    if (tid < 32) *(u32x4*)(sm + KC_U + tid * 16) = u32x4{0, 0, 0, 0};
  }
  __syncthreads();
  u32x4 B[16];
  const char* pb = sm + KC_U + (-128 - 32 * w + 16 * a + 8 * g + 240) * 2;
#pragma unroll
  for (int i = 0; i < 16; ++i) B[i] = *(const u32x4*)(pb + 256 * i);
  const char* pa = sm + ((7 - a) & 7) * KC_STR + (253 - (a >> 3) + g - 4 * w - 240) * 16;
  f32x4 acc[8];
#pragma unroll
  for (int i = 0; i < 8; ++i) acc[i] = f32x4{0.f, 0.f, 0.f, 0.f};
#pragma unroll
  for (int k = 0; k < 16; ++k) {
    const u32x4 A = *(const u32x4*)(pa + (15 - k) * 256);
#pragma unroll
    for (int i = (k >> 1); i < 8; ++i) acc[i] = mfma_h(A, B[2 * i - k + 1], acc[i]);
  }
  __syncthreads();
#pragma unroll
  for (int i = 0; i < 8; ++i) *(f32x4*)(sm + (w * 8 + i) * 1024 + lane * 16) = acc[i];
  __syncthreads();
#pragma unroll
  for (int s = 0; s < 2; ++s) {
    const int i = 2 * w + s;
    f32x4 y = *(const f32x4*)(sm + i * 1024 + lane * 16);
#pragma unroll
    for (int ww = 1; ww < 4; ++ww) y += *(const f32x4*)(sm + (ww * 8 + i) * 1024 + lane * 16);
    const int tok = tok0 + 256 * s;
    const u32x2 uh = *(const u32x2*)(sm + KC_U + 512 + tok * 2);
    const u32x2 xx = s ? xb : xa;
    u32x2 o;
#pragma unroll
    for (int e = 0; e < 2; ++e) {
      const unsigned uhe = uh[e];
      f16x2 u2 = __builtin_bit_cast(f16x2, uhe);
      unsigned yb = pkbf(y[2 * e] * kinv, y[2 * e + 1] * kinv);
      unsigned ud = pkbf((float)u2[0] * dsk, (float)u2[1] * dsk);
      unsigned sv = pkbf(blo(yb) + blo(ud), bhi(yb) + bhi(ud));
      o[e] = pkbf(blo(xx[e]) + blo(sv), bhi(xx[e]) + bhi(sv));
    }
    *(u32x2*)(ws + O_HT + ((size_t)d * SL + tok) * 2) = o;
  }
}

DEV unsigned frag_off(int t, int n, int KS) {
  return (unsigned)((((t >> 4) * KS + (n >> 5)) * 64 + ((n >> 3) & 3) * 16 + (t & 15)) * 16 + (n & 7) * 2);
}

__global__ __launch_bounds__(320, 1) void k_g1(char* __restrict__ ws) {
  __shared__ __attribute__((aligned(16))) char sm[81920 + 1024];
  const int tid = threadIdx.x, lane = tid & 63;
  const int w = __builtin_amdgcn_readfirstlane(tid >> 6);
  const int xcd = blockIdx.x & 7, jj = blockIdx.x >> 3;
  const int tt = xcd * 4 + (jj >> 3), nb = jj & 7;
  const int t0 = tt * 64, nt = nb * 5 + w, n0 = nt * 16;
  const int g = lane >> 4, i = lane & 15, qq = i >> 2, p = i & 3;
  const f32x4 cs = *(const f32x4*)((const float*)(ws + O_CS1) + n0 + 4 * g);
  const unsigned smb = lds_addr(sm);
  const unsigned vo0 = (unsigned)(lane >> 3) * 4096u + (unsigned)(((lane & 7) ^ swz128(lane >> 3)) << 4);
  const unsigned vo1 = (unsigned)(lane >> 3) * 4096u + (unsigned)(((lane & 7) ^ swz128(8 + (lane >> 3))) << 4);
  const char* hsrc = ws + O_HT + (size_t)t0 * 2 + (size_t)w * 4 * 32768;
  const char* wsrc = ws + O_W1 + (size_t)nt * 20 * 1024;
  const unsigned vl = lane * 16;
  u32x4 wf[20];
#pragma unroll
  for (int c = 0; c < 4; ++c) {
#pragma unroll
    for (int j = 0; j < 4; ++j)
      DMA16(smb + (unsigned)(20 * c + 4 * w + j) * 1024u, hsrc + (size_t)(20 * c + j) * 32768, (j & 1) ? vo1 : vo0, 0);
    GLD16(wf[5 * c + 0], wsrc + c * 5120, vl, 0);
    GLD16(wf[5 * c + 1], wsrc + c * 5120, vl, 1024);
    GLD16(wf[5 * c + 2], wsrc + c * 5120, vl, 2048);
    GLD16(wf[5 * c + 3], wsrc + c * 5120, vl, 3072);
    GLD16(wf[5 * c + 4], wsrc + c * 5120 + 4096, vl, 0);
  }
  f32x4 acc[4];
#pragma unroll
  for (int m = 0; m < 4; ++m) acc[m] = f32x4{0.f, 0.f, 0.f, 0.f};
  float s1 = 0.f, s2 = 0.f;
  const int r0 = 8 * g + qq, r1 = r0 + 4;
  unsigned ta0[4], ta1[4];
#pragma unroll
  for (int m = 0; m < 4; ++m) {
    ta0[m] = r0 * 128 + (((2 * m + (p >> 1)) ^ swz128(r0)) << 4) + 8 * (p & 1);
    ta1[m] = r1 * 128 + (((2 * m + (p >> 1)) ^ swz128(r1)) << 4) + 8 * (p & 1);
  }
  const unsigned ones = 0x3f803f80u;
#pragma unroll
  for (int c = 0; c < 4; ++c) {
    asm volatile("s_waitcnt vmcnt(%5)\n\ts_barrier"
                 : "+v"(wf[5 * c]), "+v"(wf[5 * c + 1]), "+v"(wf[5 * c + 2]), "+v"(wf[5 * c + 3]), "+v"(wf[5 * c + 4])
                 : "i"(27 - 9 * c) : "memory");
#pragma unroll
    for (int k5 = 0; k5 < 5; ++k5) {
      const int ks = 5 * c + k5;
#pragma unroll
      for (int m = 0; m < 4; ++m) {
        u32x2 b0 = tr_read(sm, ks * 4096 + ta0[m]), b1 = tr_read(sm, ks * 4096 + ta1[m]);
        u32x4 bf = {b0[0], b0[1], b1[0], b1[1]};
        acc[m] = mfma_bf(wf[ks], bf, acc[m]);
        if (w == m) {
#pragma unroll
          for (int e = 0; e < 4; ++e) { s1 = dot2bf(bf[e], ones, s1); s2 = dot2bf(bf[e], bf[e], s2); }
        }
      }
    }
  }
  f32x4* st = (f32x4*)(sm + 81920);
  if (w < 4) {
    s1 += __shfl_xor(s1, 16); s1 += __shfl_xor(s1, 32);
    s2 += __shfl_xor(s2, 16); s2 += __shfl_xor(s2, 32);
    const float mean = s1 * (1.0f / DM);
    const float var = fmaxf(s2 * (1.0f / DM) - mean * mean, 0.f);
    float mb, sb;
    ln_scalars(mean, var, mb, sb);
    const float is = 1.0f / sb;
    float mb2, sb2;
    ln_scalars((mean - mb) * is, var * is * is, mb2, sb2);
    const float R = is / sb2;
    if (g == 0) st[w * 16 + i] = f32x4{mb + mb2 * sb, R, mb, is};
  }
  __syncthreads();
  const int grp = n0 / 160;
  const int sh = (grp == 0) ? 1 : (grp == 1) ? -1 : (grp == 2) ? 2 : -2;
  const int n = n0 + 4 * g;
  const int rr = n + qq;
#pragma unroll
  for (int m = 0; m < 4; ++m) {
    const f32x4 sv = st[m * 16 + i];
    const int t = t0 + m * 16 + i;
    unsigned p01 = pkbf(sv[1] * fmaf(-sv[0], cs[0], acc[m][0]), sv[1] * fmaf(-sv[0], cs[1], acc[m][1]));
    unsigned p23 = pkbf(sv[1] * fmaf(-sv[0], cs[2], acc[m][2]), sv[1] * fmaf(-sv[0], cs[3], acc[m][3]));
    u32x2 o = {pkbf(gelu_f(blo(p01)), gelu_f(bhi(p01))), pkbf(gelu_f(blo(p23)), gelu_f(bhi(p23)))};
    const int td = t + sh;
    if (td >= 0 && td < SL) *(u32x2*)(ws + O_HB + frag_off(td, n, 20)) = o;
    const int tsrc = t - sh;
    if (tsrc < 0 || tsrc >= SL) *(u32x2*)(ws + O_HB + frag_off(t, n, 20)) = u32x2{0, 0};
    u32x2 hv = tr_read(sm, rr * 128 + (((2 * m + (p >> 1)) ^ swz128(rr)) << 4) + 8 * (p & 1));
    u32x2 zv;
#pragma unroll
    for (int e = 0; e < 2; ++e) {
      unsigned dd = pkbf(blo(hv[e]) - sv[2], bhi(hv[e]) - sv[2]);
      zv[e] = pkbf(blo(dd) * sv[3], bhi(dd) * sv[3]);
    }
    const size_t dofs = ((size_t)((tt * 4 + m) * 40 + nt) * 64 + lane) * 8;
    *(u32x2*)(ws + O_HD + dofs) = hv;
    *(u32x2*)(ws + O_ZD + dofs) = zv;
  }
}

__global__ __launch_bounds__(256, 1) void k_g2(char* __restrict__ ws) {
  __shared__ __attribute__((aligned(16))) char sm[102400];
  const int tid = threadIdx.x, lane = tid & 63;
  const int w = __builtin_amdgcn_readfirstlane(tid >> 6);
  const int xcd = blockIdx.x & 7, jj = blockIdx.x >> 3;
  const int tt = xcd * 4 + (jj >> 3), nb = jj & 7;
  const int t16 = tt * 4 + w;
  const int g = lane >> 4, i = lane & 15;
  u32x2 zv[5];
#pragma unroll
  for (int j = 0; j < 5; ++j) zv[j] = *(const u32x2*)(ws + O_ZD + ((size_t)(t16 * 40 + nb * 5 + j) * 64 + lane) * 8);
  const unsigned smb = lds_addr(sm);
  const unsigned vl = lane * 16;
  const char* asrc = ws + O_HB + (size_t)t16 * 20 * 1024;
  const char* wsrc = ws + O_W2 + (size_t)(nb * 5) * 20 * 1024;
  u32x4 af[20];
#pragma unroll
  for (int c = 0; c < 5; ++c) {
    const unsigned o1 = (unsigned)(w * 20 + 4 * c) * 1024u, o2 = (unsigned)(80 + 4 * c + w) * 1024u;
    DMA16(smb + o1, wsrc + o1, vl, 0);
    DMA16(smb + o1, wsrc + o1, vl, 1024);
    DMA16(smb + o1, wsrc + o1, vl, 2048);
    DMA16(smb + o1, wsrc + o1, vl, 3072);
    DMA16(smb + o2, wsrc + o2, vl, 0);
    GLD16(af[4 * c + 0], asrc + c * 4096, vl, 0);
    GLD16(af[4 * c + 1], asrc + c * 4096, vl, 1024);
    GLD16(af[4 * c + 2], asrc + c * 4096, vl, 2048);
    GLD16(af[4 * c + 3], asrc + c * 4096, vl, 3072);
  }
  f32x4 acc[5];
#pragma unroll
  for (int j = 0; j < 5; ++j) acc[j] = f32x4{0.f, 0.f, 0.f, 0.f};
#pragma unroll
  for (int c = 0; c < 5; ++c) {
    asm volatile("s_waitcnt vmcnt(%4)\n\ts_barrier"
                 : "+v"(af[4 * c]), "+v"(af[4 * c + 1]), "+v"(af[4 * c + 2]), "+v"(af[4 * c + 3])
                 : "i"(36 - 9 * c) : "memory");
#pragma unroll
    for (int k4 = 0; k4 < 4; ++k4) {
      const int ks = 4 * c + k4;
#pragma unroll
      for (int j = 0; j < 5; ++j)
        acc[j] = mfma_bf(*(const u32x4*)(sm + (j * 20 + ks) * 1024 + vl), af[ks], acc[j]);
    }
  }
  const int t = t16 * 16 + i;
#pragma unroll
  for (int j = 0; j < 5; ++j) {
    const int n = (nb * 5 + j) * 16 + 4 * g;
    unsigned h01 = pkbf(acc[j][0], acc[j][1]), h23 = pkbf(acc[j][2], acc[j][3]);
    u32x2 o = {pkbf(blo(zv[j][0]) + blo(h01), bhi(zv[j][0]) + bhi(h01)),
               pkbf(blo(zv[j][1]) + blo(h23), bhi(zv[j][1]) + bhi(h23))};
    *(u32x2*)(ws + O_X1 + frag_off(t, n, 20)) = o;
  }
}

__global__ __launch_bounds__(512, 2) void k_g3(char* __restrict__ ws) {
  __shared__ __attribute__((aligned(16))) char sm[143360];
  const int tid = threadIdx.x, lane = tid & 63;
  const int w = __builtin_amdgcn_readfirstlane(tid >> 6);
  const int xcd = blockIdx.x & 7, jj = blockIdx.x >> 3;
  const int tb = (xcd >> 1) * 4 + (jj >> 3), nbk = (xcd & 1) * 8 + (jj & 7);
  const int nt0 = (nbk * 101) / 15;
  const int t16 = tb * 8 + w;
  const int g = lane >> 4, i = lane & 15;
  f32x4 cs[7];
#pragma unroll
  for (int j = 0; j < 7; ++j) cs[j] = *(const f32x4*)((const float*)(ws + O_CS3) + (nt0 + j) * 16 + 4 * g);
  const unsigned smb = lds_addr(sm);
  const unsigned vl = lane * 16;
  const char* asrc = ws + O_X1 + (size_t)t16 * 20 * 1024;
  const char* wsrc = ws + O_W3 + (size_t)nt0 * 20 * 1024;
  unsigned vo[4];
#pragma unroll
  for (int j = 0; j < 4; ++j) {
    const unsigned pos = (unsigned)w * 3584u + j * 1024u + vl;
    const unsigned f = pos >> 10;
    vo[j] = (f >> 2) * 20480u + (f & 3) * 1024u + (pos & 1023u);
  }
  u32x4 af[20];
#pragma unroll
  for (int c = 0; c < 5; ++c) {
    const unsigned lo = smb + (unsigned)c * 28672u + (unsigned)w * 3584u;
    DMA16(lo, wsrc + c * 4096, vo[0], 0);
    DMA16(lo + 1024u, wsrc + c * 4096, vo[1], 0);
    DMA16(lo + 2048u, wsrc + c * 4096, vo[2], 0);
    if (lane < 32) DMA16(lo + 3072u, wsrc + c * 4096, vo[3], 0);
    GLD16(af[4 * c + 0], asrc + c * 4096, vl, 0);
    GLD16(af[4 * c + 1], asrc + c * 4096, vl, 1024);
    GLD16(af[4 * c + 2], asrc + c * 4096, vl, 2048);
    GLD16(af[4 * c + 3], asrc + c * 4096, vl, 3072);
  }
  f32x4 acc[7];
#pragma unroll
  for (int j = 0; j < 7; ++j) acc[j] = f32x4{0.f, 0.f, 0.f, 0.f};
  float s1 = 0.f, s2 = 0.f;
  const unsigned ones = 0x3f803f80u;
#pragma unroll
  for (int c = 0; c < 5; ++c) {
    asm volatile("s_waitcnt vmcnt(%4)\n\ts_barrier"
                 : "+v"(af[4 * c]), "+v"(af[4 * c + 1]), "+v"(af[4 * c + 2]), "+v"(af[4 * c + 3])
                 : "i"(32 - 8 * c) : "memory");
#pragma unroll
    for (int k4 = 0; k4 < 4; ++k4) {
      const int ks = 4 * c + k4;
#pragma unroll
      for (int j = 0; j < 7; ++j)
        acc[j] = mfma_bf(*(const u32x4*)(sm + c * 28672 + (j * 4 + k4) * 1024 + vl), af[ks], acc[j]);
#pragma unroll
      for (int e = 0; e < 4; ++e) { s1 = dot2bf(af[ks][e], ones, s1); s2 = dot2bf(af[ks][e], af[ks][e], s2); }
    }
  }
  s1 += __shfl_xor(s1, 16); s1 += __shfl_xor(s1, 32);
  s2 += __shfl_xor(s2, 16); s2 += __shfl_xor(s2, 32);
  const float mean = s1 * (1.0f / DM);
  const float var = fmaxf(s2 * (1.0f / DM) - mean * mean, 0.f);
  float mb, sb;
  ln_scalars(mean, var, mb, sb);
  const float is = 1.0f / sb;
  const int t = t16 * 16 + i;
#pragma unroll
  for (int j = 0; j < 7; ++j) {
    const int n = (nt0 + j) * 16 + 4 * g;
    unsigned p01 = pkbf(is * fmaf(-mb, cs[j][0], acc[j][0]), is * fmaf(-mb, cs[j][1], acc[j][1]));
    unsigned p23 = pkbf(is * fmaf(-mb, cs[j][2], acc[j][2]), is * fmaf(-mb, cs[j][3], acc[j][3]));
    u32x2 o = {pkbf(gelu_f(blo(p01)), gelu_f(bhi(p01))), pkbf(gelu_f(blo(p23)), gelu_f(bhi(p23)))};
    *(u32x2*)(ws + O_A3 + frag_off(t, n, 54)) = o;
  }
}

__global__ __launch_bounds__(256, 1) void k_g4(char* __restrict__ ws, u16* __restrict__ out) {
  __shared__ __attribute__((aligned(16))) char sm[5 * 30720];
  const int tid = threadIdx.x, lane = tid & 63;
  const int w = __builtin_amdgcn_readfirstlane(tid >> 6);
  const int xcd = blockIdx.x & 7, jj = blockIdx.x >> 3;
  const int tt = xcd * 4 + (jj >> 3), nb = jj & 7;
  const int t16 = tt * 4 + w;
  const int g = lane >> 4, i = lane & 15;
  const int t = t16 * 16 + i;
  u32x2 hv[5], xv[5];
#pragma unroll
  for (int j = 0; j < 5; ++j) {
    hv[j] = *(const u32x2*)(ws + O_HD + ((size_t)(t16 * 40 + nb * 5 + j) * 64 + lane) * 8);
    xv[j] = *(const u32x2*)(ws + O_X1 + frag_off(t, (nb * 5 + j) * 16 + 4 * g, 20));
  }
  const unsigned smb = lds_addr(sm);
  const unsigned vl = lane * 16;
  const char* asrc = ws + O_A3 + (size_t)t16 * 54 * 1024;
  const char* wsrc = ws + O_W4 + (size_t)(nb * 5) * 54 * 1024;
  unsigned vo[8];
#pragma unroll
  for (int j = 0; j < 8; ++j) {
    const unsigned pos = (unsigned)w * 7680u + j * 1024u + vl;
    const unsigned f = pos >> 10;
    const unsigned ntl = (f * 43u) >> 8;
    vo[j] = ntl * (54u * 1024u) + (f - ntl * 6u) * 1024u + (pos & 1023u);
  }
  u32x4 af[54];
#define G4_ISSUE(c)                                                              \
  do {                                                                           \
    const unsigned lo_ = smb + (unsigned)((c) % 5) * 30720u + (unsigned)w * 7680u; \
    _Pragma("unroll") for (int j_ = 0; j_ < 7; ++j_) DMA16(lo_ + j_ * 1024u, wsrc + (c) * 6144, vo[j_], 0); \
    if (lane < 32) DMA16(lo_ + 7168u, wsrc + (c) * 6144, vo[7], 0);              \
    GLD16(af[6 * (c) + 0], asrc + (c) * 6144, vl, 0);                            \
    GLD16(af[6 * (c) + 1], asrc + (c) * 6144, vl, 1024);                         \
    GLD16(af[6 * (c) + 2], asrc + (c) * 6144, vl, 2048);                         \
    GLD16(af[6 * (c) + 3], asrc + (c) * 6144, vl, 3072);                         \
    GLD16(af[6 * (c) + 4], asrc + (c) * 6144 + 4096, vl, 0);                     \
    GLD16(af[6 * (c) + 5], asrc + (c) * 6144 + 4096, vl, 1024);                  \
  } while (0)
#pragma unroll
  for (int c = 0; c < 4; ++c) G4_ISSUE(c);
  f32x4 acc[5];
#pragma unroll
  for (int j = 0; j < 5; ++j) acc[j] = f32x4{0.f, 0.f, 0.f, 0.f};
#pragma unroll
  for (int c = 0; c < 9; ++c) {
    asm volatile("s_waitcnt vmcnt(%6) lgkmcnt(0)\n\ts_barrier"
                 : "+v"(af[6 * c]), "+v"(af[6 * c + 1]), "+v"(af[6 * c + 2]), "+v"(af[6 * c + 3]), "+v"(af[6 * c + 4]),
                   "+v"(af[6 * c + 5])
                 : "i"(14 * ((8 - c) < 3 ? (8 - c) : 3)) : "memory");
    if (c + 4 < 9) G4_ISSUE(c + 4);
#pragma unroll
    for (int k6 = 0; k6 < 6; ++k6) {
#pragma unroll
      for (int j = 0; j < 5; ++j)
        acc[j] = mfma_bf(*(const u32x4*)(sm + (c % 5) * 30720 + (j * 6 + k6) * 1024 + vl), af[6 * c + k6], acc[j]);
    }
  }
#pragma unroll
  for (int j = 0; j < 5; ++j) {
    const int n = (nb * 5 + j) * 16 + 4 * g;
    unsigned h01 = pkbf(acc[j][0], acc[j][1]), h23 = pkbf(acc[j][2], acc[j][3]);
    unsigned b01 = pkbf(blo(xv[j][0]) + blo(h01), bhi(xv[j][0]) + bhi(h01));
    unsigned b23 = pkbf(blo(xv[j][1]) + blo(h23), bhi(xv[j][1]) + bhi(h23));
    u32x2 o = {pkbf(blo(hv[j][0]) + blo(b01), bhi(hv[j][0]) + bhi(b01)),
               pkbf(blo(hv[j][1]) + blo(b23), bhi(hv[j][1]) + bhi(b23))};
    *(u32x2*)(out + (size_t)t * DM + n) = o;
  }
}

extern "C" void kernel_launch(void* const* d_in, const int* in_sizes, int n_in, void* d_out, int out_size, void* d_ws,
                              size_t ws_size, hipStream_t stream) {
  (void)in_sizes; (void)n_in; (void)out_size; (void)ws_size;
  const u16* X = (const u16*)d_in[0];
  char* ws = (char*)d_ws;
  k_prep<<<NB_LN + NB_CS + NB_PK + NB_KG, 256, 0, stream>>>(
      X, (const u16*)d_in[1], (const u16*)d_in[2], (const u16*)d_in[3], (const u16*)d_in[4], (const u16*)d_in[5],
      (const u16*)d_in[8], (const u16*)d_in[9], (const u16*)d_in[10], (const u16*)d_in[11], ws);
  k_conv<<<DM, 256, 0, stream>>>(ws, (const u16*)d_in[6]);
  k_g1<<<256, 320, 0, stream>>>(ws);
  k_g2<<<256, 256, 0, stream>>>(ws);
  k_g3<<<256, 512, 0, stream>>>(ws);
  k_g4<<<256, 256, 0, stream>>>(ws, (u16*)d_out);
}
```

```cpp
#include <hip/hip_runtime.h>
#include <hip/hip_bf16.h>
#include <stdint.h>

typedef unsigned short u16;
typedef __attribute__((ext_vector_type(4))) unsigned u32x4;
typedef __attribute__((ext_vector_type(2))) unsigned u32x2;
typedef __attribute__((ext_vector_type(4))) float f32x4;
typedef __attribute__((ext_vector_type(2))) float f32x2;
typedef __attribute__((ext_vector_type(8))) __bf16 bf16x8;
typedef __attribute__((ext_vector_type(2))) __bf16 bf16x2;
typedef __attribute__((ext_vector_type(8))) _Float16 f16x8;
typedef __attribute__((ext_vector_type(2))) _Float16 f16x2;
typedef __attribute__((ext_vector_type(4))) short s16x4;
#define LDSP __attribute__((address_space(3)))
#define DEV __device__ __forceinline__
#define SCHED_FENCE() __builtin_amdgcn_sched_barrier(0)

constexpr int SL = 2048, DM = 640, NS = 128, FF = 1728;
constexpr int KRW = 2064;

constexpr size_t al4k(size_t x) { return (x + 4095) & ~(size_t)4095; }
constexpr size_t ACT = (size_t)SL * DM * 2;
constexpr size_t O_XU = 0;
constexpr size_t O_KR = O_XU + al4k(2 * ACT);
constexpr size_t O_KS = O_KR + al4k((size_t)DM * KRW * 2);
constexpr size_t O_HT = O_KS + 4096;
constexpr size_t O_W1 = O_HT + al4k(ACT);
constexpr size_t O_W2 = O_W1 + al4k((size_t)DM * DM * 2);
constexpr size_t O_W3 = O_W2 + al4k((size_t)DM * DM * 2);
constexpr size_t O_W4 = O_W3 + al4k((size_t)DM * FF * 2);
constexpr size_t O_CS1 = O_W4 + al4k((size_t)DM * FF * 2);
constexpr size_t O_CS3 = O_CS1 + 4096;
constexpr size_t O_ST = O_CS3 + 8192;
constexpr size_t O_S2 = O_ST + 16384;
constexpr size_t O_CNT = O_S2 + 131072;
constexpr size_t O_SY = O_CNT + 2 * 49152;
constexpr size_t O_HB = O_SY + 16384;
constexpr size_t O_HF = O_HB + al4k(ACT);
constexpr size_t O_X1 = O_HF + al4k(ACT);
constexpr size_t O_A3 = O_X1 + al4k(ACT);
constexpr size_t O_END = O_A3 + al4k((size_t)SL * FF * 2);

DEV float bf2f(u16 v) { return __uint_as_float((unsigned)v << 16); }
DEV float blo(unsigned v) { return __uint_as_float(v << 16); }
DEV float bhi(unsigned v) { return __uint_as_float(v & 0xffff0000u); }
DEV f32x2 bup(unsigned v) { return f32x2{blo(v), bhi(v)}; }
DEV unsigned pkbf2(f32x2 f) { return __builtin_bit_cast(unsigned, __builtin_convertvector(f, bf16x2)); }
DEV unsigned pkbf(float a, float b) { return pkbf2(f32x2{a, b}); }
DEV unsigned pkh2(f32x2 f) { return __builtin_bit_cast(unsigned, __builtin_convertvector(f, f16x2)); }
DEV float rbf(float a) { return blo(pkbf(a, a)); }
DEV f32x2 fma2(f32x2 a, f32x2 b, f32x2 c) { return __builtin_elementwise_fma(a, b, c); }
DEV f32x2 bc2(float a) { return f32x2{a, a}; }
DEV unsigned gelu2(f32x2 y, float c1, float c2, float sb) {
  f32x2 t = y * fma2(y * y, bc2(c2), bc2(c1));
  f32x2 e = {__builtin_amdgcn_exp2f(t.x), __builtin_amdgcn_exp2f(t.y)};
  f32x2 d = fma2(e, bc2(sb), bc2(sb));
  f32x2 r = {__builtin_amdgcn_rcpf(d.x), __builtin_amdgcn_rcpf(d.y)};
  return pkbf2(y * r);
}
constexpr float GC1 = -2.0f * 0.7978845608028654f * 1.4426950408889634f;
constexpr float GC2 = GC1 * 0.044715f;

DEV u32x2 tr_read(const char* lds_base, unsigned off) {
  s16x4 v = __builtin_amdgcn_ds_read_tr16_b64_v4i16((LDSP s16x4*)(lds_base + off));
  return __builtin_bit_cast(u32x2, v);
}
DEV f32x4 mfma_bf(u32x4 a, u32x4 b, f32x4 c) {
  return __builtin_amdgcn_mfma_f32_16x16x32_bf16(__builtin_bit_cast(bf16x8, a), __builtin_bit_cast(bf16x8, b), c, 0, 0, 0);
}
DEV f32x4 mfma_h(u32x4 a, u32x4 b, f32x4 c) {
  return __builtin_amdgcn_mfma_f32_16x16x32_f16(__builtin_bit_cast(f16x8, a), __builtin_bit_cast(f16x8, b), c, 0, 0, 0);
}
DEV float dot2bf(unsigned a, unsigned b, float c) {
  return __builtin_amdgcn_fdot2_f32_bf16(__builtin_bit_cast(bf16x2, a), __builtin_bit_cast(bf16x2, b), c, false);
}
DEV unsigned lds_addr(const void* p) { return (unsigned)(size_t)(LDSP const char*)p; }
template <int CTRL> DEV float dppf(float v) {
  return __builtin_bit_cast(float, __builtin_amdgcn_update_dpp(0, __builtin_bit_cast(int, v), CTRL, 0xf, 0xf, true));
}
DEV float sum16(float s) {
  s += dppf<0xB1>(s); s += dppf<0x4E>(s); s += dppf<0x141>(s); s += dppf<0x140>(s);
  return s;
}
DEV float sum_rows(float s) {
  unsigned u = __float_as_uint(s);
  auto a = __builtin_amdgcn_permlane16_swap(u, u, false, false);
  s = __uint_as_float(a[0]) + __uint_as_float(a[1]);
  u = __float_as_uint(s);
  auto b = __builtin_amdgcn_permlane32_swap(u, u, false, false);
  return __uint_as_float(b[0]) + __uint_as_float(b[1]);
}
DEV void st16(void* p, u32x4 v) { asm volatile("global_store_dwordx4 %0, %1, off sc1\n\ts_nop 1" ::"v"(p), "v"(v) : "memory"); }
DEV void st8(void* p, u32x2 v) { asm volatile("global_store_dwordx2 %0, %1, off sc1" ::"v"(p), "v"(v) : "memory"); }
DEV void st4(void* p, unsigned v) { asm volatile("global_store_dword %0, %1, off sc1" ::"v"(p), "v"(v) : "memory"); }

#define DMA16(lds, g, v, OFF)                                                                          \
  do {                                                                                                 \
    unsigned keep_;                                                                                    \
    asm volatile("s_mov_b32 %0, m0\n\ts_mov_b32 m0, %1\n\ts_nop 0\n\t"                                 \
                 "global_load_lds_dwordx4 %2, %3 offset:%4\n\ts_mov_b32 m0, %0"                        \
                 : "=&s"(keep_) : "s"(lds), "v"(v), "s"(g), "i"(OFF) : "memory");                      \
  } while (0)
#define DMA16H(lds, g, v)                                                                              \
  do {                                                                                                 \
    unsigned keep_;                                                                                    \
    asm volatile("s_mov_b32 %0, m0\n\ts_mov_b32 m0, %1\n\ts_mov_b64 exec, 0xffffffff\n\t"              \
                 "global_load_lds_dwordx4 %2, %3\n\ts_mov_b64 exec, -1\n\ts_mov_b32 m0, %0"            \
                 : "=&s"(keep_) : "s"(lds), "v"(v), "s"(g) : "memory");                                \
  } while (0)
#define DMA16x4(lds, g, v)                                                                             \
  do {                                                                                                 \
    unsigned keep_;                                                                                    \
    asm volatile("s_mov_b32 %0, m0\n\ts_mov_b32 m0, %1\n\ts_nop 0\n\t"                                 \
                 "global_load_lds_dwordx4 %2, %3\n\tglobal_load_lds_dwordx4 %2, %3 offset:1024\n\t"    \
                 "global_load_lds_dwordx4 %2, %3 offset:2048\n\tglobal_load_lds_dwordx4 %2, %3 offset:3072\n\t" \
                 "s_mov_b32 m0, %0"                                                                    \
                 : "=&s"(keep_) : "s"(lds), "v"(v), "s"(g) : "memory");                                \
  } while (0)
#define GLD8S(dst, g, v) asm volatile("global_load_dwordx2 %0, %1, %2 sc1" : "=v"(dst) : "v"(v), "s"(g) : "memory")
#define GLD16S(dst, g, v, OFF) \
  asm volatile("global_load_dwordx4 %0, %1, %2 offset:%3 sc1" : "=v"(dst) : "v"(v), "s"(g), "i"(OFF) : "memory")
#define GLD8(dst, g, v) asm volatile("global_load_dwordx2 %0, %1, %2" : "=v"(dst) : "v"(v), "s"(g) : "memory")
#define GLD16(dst, g, v, OFF) \
  asm volatile("global_load_dwordx4 %0, %1, %2 offset:%3" : "=v"(dst) : "v"(v), "s"(g), "i"(OFF) : "memory")
#define WARM(lds, g, v) DMA16(lds, g, v, 0)

#define STAMP(K, i)
#define STAMP_DECL
#define STAMP_ARG
#define STAMP_PASS
#define STAMP_FLUSH(K, ws)

DEV void ln_scalars(float mean, float var, float& mb, float& sb) {
  mb = rbf(mean);
  sb = rbf(__builtin_amdgcn_sqrtf(rbf(rbf(var) + 1e-5f)));
}

constexpr int NB_LN = 128, NB_KG = 320, NB_PK1 = 25, NB_CS1 = 10, NB_PK2 = 80, NB_CS2 = 27;

DEV unsigned ld_agent(const char* p) { return __hip_atomic_load((const unsigned*)p, __ATOMIC_RELAXED, __HIP_MEMORY_SCOPE_AGENT); }
DEV void st_agent(char* p, unsigned v) { __hip_atomic_store((unsigned*)p, v, __ATOMIC_RELAXED, __HIP_MEMORY_SCOPE_AGENT); }
DEV unsigned epoch_tag(unsigned e0) { return e0 * 0x9E3779B1u + 0x7F4A7C15u; }
constexpr size_t SY_A = 1024, SY_R = 2048, SY_D = SY_R + 32 * 128;

DEV void role_ln(int b, const u16* __restrict__ X, char* __restrict__ ws, char* sm STAMP_ARG) {
  const int tid = threadIdx.x;
  __builtin_amdgcn_s_setprio(3);
  const unsigned e0 = ld_agent(ws + O_SY);
  const int tile = b;
  const int t0 = tile * 16, r = tid >> 4, sub = tid & 15;
  const u32x4* src = (const u32x4*)(X + (size_t)(t0 + r) * DM);
  u32x4 v[5];
#pragma unroll
  for (int j = 0; j < 5; ++j) v[j] = src[j * 16 + sub];
  const int fr = ((r & 3) | ((r >> 3) << 2)) << 1;
  float s = 0.f;
#pragma unroll
  for (int j = 0; j < 5; ++j) {
    *(u32x4*)(sm + r * 1280 + (((j * 16 + sub) ^ fr) << 4)) = v[j];
#pragma unroll
    for (int e = 0; e < 4; ++e) s = dot2bf(v[j][e], 0x3f803f80u, s);
  }
  STAMP(0, 8);
  const float mean = sum16(s) * (1.0f / DM);
  f32x2 q2 = {0.f, 0.f};
  f32x2 xf[20];
#pragma unroll
  for (int j = 0; j < 5; ++j)
#pragma unroll
    for (int e = 0; e < 4; ++e) {
      xf[j * 4 + e] = bup(v[j][e]);
      f32x2 a = xf[j * 4 + e] - bc2(mean);
      q2 = fma2(a, a, q2);
    }
  const float var = sum16(q2.x + q2.y) * (1.0f / DM);
  float mb, sb;
  ln_scalars(mean, var, mb, sb);
  const float is = __builtin_amdgcn_rcpf(sb);
#pragma unroll
  for (int j = 0; j < 5; ++j) {
    u32x4 o;
#pragma unroll
    for (int e = 0; e < 4; ++e) {
      unsigned dd = pkbf2(xf[j * 4 + e] - bc2(mb));
      unsigned uu = pkbf2(bup(dd) * bc2(is));
      o[e] = pkh2(bup(uu));
    }
    *(u32x4*)(sm + 20480 + r * 1280 + (((j * 16 + sub) ^ fr) << 4)) = o;
  }
  STAMP(0, 9);
  __syncthreads();
  STAMP(0, 10);
  const int lane = tid & 63, w = tid >> 6, g = lane >> 4, i = lane & 15, qq = i >> 2, p = i & 3;
  const int r0 = (g & 1) * 8 + qq;
  const int f0 = ((r0 & 3) | ((r0 >> 3) << 2)) << 1;
  const char* src2 = sm + (g >> 1) * 20480 + r0 * 1280 + 8 * (p & 1);
  char* go = ws + O_XU + ((size_t)tile * DM + i) * 64 + g * 16;
#pragma unroll
  for (int it = 0; it < 10; ++it) {
    const int cb = it * 4 + w;
    const unsigned a0 = (unsigned)(((2 * cb + (p >> 1)) ^ f0) << 4);
    u32x2 x0 = tr_read(src2, a0), x1 = tr_read(src2, a0 + 5120);
    st16(go + cb * 1024, u32x4{x0[0], x0[1], x1[0], x1[1]});
  }
  asm volatile("s_waitcnt vmcnt(0)\n\ts_barrier" ::: "memory");
  if (tid == 0) st_agent(ws + O_SY + SY_A + b * 4, epoch_tag(e0));
}

DEV void role_collect(char* __restrict__ ws) {
  const int lane = threadIdx.x;
  char* sy = ws + O_SY;
  const unsigned tag = epoch_tag(ld_agent(sy));
  if (lane < 16) st_agent(sy + SY_D + lane * 128, 0u);
  asm volatile("s_waitcnt vmcnt(0)" ::: "memory");
  for (unsigned spin = 0; spin < (1u << 18); ++spin) {
    const unsigned a = ld_agent(sy + SY_A + lane * 4), c = ld_agent(sy + SY_A + 256 + lane * 4);
    if (__all(a == tag && c == tag)) break;
    __builtin_amdgcn_s_sleep(2);
  }
  if (lane < 32) st_agent(sy + SY_R + lane * 128, tag);
  for (unsigned spin = 0; spin < (1u << 18); ++spin) {
    __builtin_amdgcn_s_sleep(32);
    const unsigned v = lane < 16 ? ld_agent(sy + SY_D + lane * 128) : 48u;
    if (__all(v >= 48u)) break;
  }
  if (lane == 0) st_agent(sy, tag);
}

struct cw { f32x2 v, s; };
DEV f32x2 cmul(f32x2 a, const cw& b) { return fma2(f32x2{a.y, a.y}, b.s, f32x2{a.x, a.x} * b.v); }
DEV cw mkw(f32x2 v) { return cw{v, f32x2{-v.y, v.x}}; }
DEV cw csq(const cw& a) { return mkw(cmul(a.v, a)); }
DEV float exp_acc(float x) {
  const float L2E = 1.4426950408889634f, L2EL = (float)(1.4426950408889634074 - (double)1.4426950408889634f);
  float hi = x * L2E;
  float lo = fmaf(x, L2E, -hi) + x * L2EL;
  float r = __builtin_amdgcn_exp2f(hi);
  return fmaf(r * lo, 0.6931471805599453f, r);
}

DEV int swz128(int row) { return (((row >> 1) & 1) | (((row >> 3) & 1) << 1)) << 1; }

constexpr unsigned long long SPLIT5 = 0x0000000000021084ull;
constexpr unsigned long long SPLIT7 = (1ull << 3) | (1ull << 9) | (1ull << 16) | (1ull << 23) | (1ull << 29) | (1ull << 36) | (1ull << 43) | (1ull << 50);
DEV void role_pack(int b, const u16* __restrict__ w1, const u16* __restrict__ w2, const u16* __restrict__ w3,
                   const u16* __restrict__ w4, char* __restrict__ ws, char* sm) {
  const int tid = threadIdx.x, lane = tid & 63, w = tid >> 6;
  char* my = sm + w * 8192;
  char* dst[2];
  int kst[2], perm[2];
  u32x4 v[2][4];
#pragma unroll
  for (int s = 0; s < 2; ++s) {
    int un = (b * 4 + w) * 2 + s;
    const u16* W; char* P; int N, KS, ns;
    const bool is3 = un >= 400 && un < 940;
    if (un < 200) { W = w1; P = ws + O_W1; N = DM; KS = 20; ns = 10; }
    else if (un < 400) { un -= 200; W = w2; P = ws + O_W2; N = DM; KS = 20; ns = 10; }
    else if (un < 940) { un -= 400; W = w3; P = ws + O_W3; N = FF; KS = 20; ns = 27; }
    else { un -= 940; W = w4; P = ws + O_W4; N = DM; KS = 54; ns = 10; }
    const int ks = un / ns, st = un - ks * ns;
    const u16* src = W + (size_t)(ks * 32) * N + st * 64;
    dst[s] = P + ((size_t)(st * 4) * KS + ks) * 1024;
    kst[s] = KS;
    perm[s] = (int)(((is3 ? SPLIT7 : SPLIT5) >> (2 * st)) & 3);
#pragma unroll
    for (int j = 0; j < 4; ++j)
      v[s][j] = *(const u32x4*)(src + (size_t)(j * 8 + (lane >> 3)) * N + (lane & 7) * 8);
  }
#pragma unroll
  for (int s = 0; s < 2; ++s)
#pragma unroll
    for (int j = 0; j < 4; ++j) {
      const int row = j * 8 + (lane >> 3);
      *(u32x4*)(my + s * 4096 + row * 128 + (((lane & 7) ^ swz128(row)) << 4)) = v[s][j];
    }
  const int g = lane >> 4, i = lane & 15, qq = i >> 2, p = i & 3;
  const int r0 = 8 * g + qq, sw = swz128(r0);
#pragma unroll
  for (int s = 0; s < 2; ++s)
#pragma unroll
    for (int nt = 0; nt < 4; ++nt) {
      const bool nat = (perm[s] >> (nt >> 1)) & 1;
      const int ch = nat ? 2 * nt + (p >> 1) : (nt >> 1) * 4 + p;
      const int hb = nat ? (p & 1) : (nt & 1);
      const unsigned a0 = s * 4096 + r0 * 128 + ((ch ^ sw) << 4) + 8 * hb;
      u32x2 a = tr_read(my, a0), c = tr_read(my, a0 + 512);
      st16(dst[s] + (size_t)nt * kst[s] * 1024 + lane * 16, u32x4{a[0], a[1], c[0], c[1]});
    }
}

DEV void role_colsum(int b, const u16* __restrict__ w1, const u16* __restrict__ w3, char* __restrict__ ws, char* sm) {
  const int tid = threadIdx.x;
  const u16* W; char* out; int N;
  if (b < 10) { W = w1 + b * 64; out = ws + O_CS1 + b * 256; N = DM; }
  else { W = w3 + (b - 10) * 64; out = ws + O_CS3 + (b - 10) * 256; N = FF; }
  const int r = tid >> 3, c = tid & 7;
  u32x4 v[20];
#pragma unroll
  for (int j = 0; j < 20; ++j) v[j] = *(const u32x4*)(W + (size_t)(j * 32 + r) * N + c * 8);
  f32x2 a[4];
#pragma unroll
  for (int e = 0; e < 4; ++e) a[e] = f32x2{0.f, 0.f};
#pragma unroll
  for (int j = 0; j < 20; ++j)
#pragma unroll
    for (int e = 0; e < 4; ++e) a[e] += bup(v[j][e]);
  float* red = (float*)sm;
#pragma unroll
  for (int e = 0; e < 4; ++e) { red[r * 65 + c * 8 + 2 * e] = a[e].x; red[r * 65 + c * 8 + 2 * e + 1] = a[e].y; }
  __syncthreads();
  if (tid < 64) {
    float s = 0.f;
#pragma unroll
    for (int j = 0; j < 32; ++j) s += red[j * 65 + tid];
    st4(out + tid * 4, __float_as_uint(s));
  }
}

constexpr int KC_STR = 4160, KC_U = 24576, KC_X = KC_U + 4608;
constexpr int KG_T = 33280;

template <int MODE> DEV bool cv_has(int i) { return MODE == 0 || ((MODE == 1) == (i == 0 || i == 3 || i == 4 || i == 7)); }
template <int MODE> DEV int cv_rank(int i) { return MODE == 1 ? (i == 0 ? 0 : i == 3 ? 1 : i == 4 ? 2 : 3) : (i == 1 ? 0 : i == 2 ? 1 : i == 5 ? 2 : 3); }
template <int MODE> DEV int cv_tile(int r) { return MODE == 1 ? (r == 0 ? 0 : r == 1 ? 3 : r == 2 ? 4 : 7) : (r == 0 ? 1 : r == 1 ? 2 : r == 2 ? 5 : 6); }

template <int MODE>
DEV void conv_body(int d, int w, int lane, float kinv, float dsk, char* __restrict__ ws, char* sm STAMP_ARG) {
  const int a = lane & 15, g = lane >> 4;
  const unsigned pa = lds_addr(sm) + ((3 - a) & 3) * KC_STR + (507 - (a >> 2) + 2 * g - 8 * w - 480) * 8;
  const unsigned pb = lds_addr(sm) + KC_U + (-128 - 32 * w + 16 * a + 8 * g + 240) * 2;
  u32x2 Al[16], Ah[16];
  u32x4 B[16];
#define CV_LA(k)                                                                                          \
  asm volatile("ds_read_b64 %0, %2 offset:%3\n\tds_read_b64 %1, %2 offset:%4"                             \
               : "=&v"(Al[k]), "=&v"(Ah[k]) : "v"(pa), "i"((15 - (k)) * 256), "i"((15 - (k)) * 256 + 8) : "memory")
#define CV_LB(i) asm volatile("ds_read_b128 %0, %1 offset:%2" : "=v"(B[i]) : "v"(pb), "i"(256 * (i)) : "memory")
  CV_LA(0);
#pragma unroll
  for (int i = 1; i < 16; i += 2) CV_LB(i);
  CV_LA(1);
#pragma unroll
  for (int i = 0; i < 16; i += 2) CV_LB(i);
  CV_LA(2);
  f32x4 acc[8];
#pragma unroll
  for (int i = 0; i < 8; ++i) acc[i] = f32x4{0.f, 0.f, 0.f, 0.f};
#pragma unroll
  for (int k = 0; k < 16; ++k) {
    if (k == 0)
      asm volatile("s_waitcnt lgkmcnt(12)" : "+v"(Al[0]), "+v"(Ah[0]), "+v"(B[1]), "+v"(B[3]), "+v"(B[5]), "+v"(B[7]), "+v"(B[9]),
                   "+v"(B[11]), "+v"(B[13]), "+v"(B[15])::"memory");
    else if (k == 1)
      asm volatile("s_waitcnt lgkmcnt(6)" : "+v"(Al[1]), "+v"(Ah[1]), "+v"(B[0]), "+v"(B[2]), "+v"(B[4]), "+v"(B[6]), "+v"(B[8]),
                   "+v"(B[10]), "+v"(B[12]), "+v"(B[14])::"memory");
    else {
      const int up = 2 * k + 2 < 15 ? 2 * k + 2 : 15, yn = 2 * (up - k);
      asm volatile("s_waitcnt lgkmcnt(%2)" : "+v"(Al[k]), "+v"(Ah[k]) : "i"(yn < 15 ? yn : 15) : "memory");
    }
    const u32x4 A = {Al[k][0], Al[k][1], Ah[k][0], Ah[k][1]};
    if (2 * k + 3 < 16) CV_LA(2 * k + 3 < 16 ? 2 * k + 3 : 15);
    if (2 * k + 4 < 16) CV_LA(2 * k + 4 < 16 ? 2 * k + 4 : 15);
#pragma unroll
    for (int i = (k >> 1); i < 8; ++i)
      if (cv_has<MODE>(i)) acc[i] = mfma_h(A, B[2 * i - k + 1], acc[i]);
    SCHED_FENCE();
  }
  STAMP(1, 8);
  __syncthreads();
  f32x4 y[2];
  char* red = sm + lane * 16;
#define CV_PUT(W)                                                                                        \
  _Pragma("unroll") for (int i_ = 0; i_ < 8; ++i_) {                                                     \
    if (!cv_has<MODE>(i_)) continue;                                                                     \
    const int ow_ = MODE == 0 ? (i_ >> 1) : cv_rank<MODE>(i_);                                           \
    if (ow_ == (W)) y[MODE == 0 ? (i_ & 1) : 0] = acc[i_];                                               \
    else *(f32x4*)(red + (i_ * 3 + ((W) < ow_ ? (W) : (W) - 1)) * 1024) = acc[i_];                       \
  }
  if (w == 0) { CV_PUT(0) } else if (w == 1) { CV_PUT(1) } else if (w == 2) { CV_PUT(2) } else { CV_PUT(3) }
  __syncthreads();
  STAMP(1, 9);
  u32x2 o[2];
#pragma unroll
  for (int s = 0; s < (MODE == 0 ? 2 : 1); ++s) {
    const int ti = MODE == 0 ? 2 * w + s : cv_tile<MODE>(w);
    const int tok = 256 * ti + 16 * a + 4 * g;
    const char* rp = red + ti * 3072;
    y[s] += *(const f32x4*)(rp) + *(const f32x4*)(rp + 1024) + *(const f32x4*)(rp + 2048);
    const u32x2 uh = *(const u32x2*)(sm + KC_U + 512 + tok * 2);
    const u32x2 xx = *(const u32x2*)(sm + KC_X + tok * 2);
#pragma unroll
    for (int e = 0; e < 2; ++e) {
      const unsigned uhe = uh[e];
      f16x2 u2 = __builtin_bit_cast(f16x2, uhe);
      unsigned yb = pkbf2(f32x2{y[s][2 * e], y[s][2 * e + 1]} * bc2(kinv));
      unsigned ud = pkbf2(f32x2{(float)u2[0], (float)u2[1]} * bc2(dsk));
      unsigned sv = pkbf2(bup(yb) + bup(ud));
      o[s][e] = pkbf2(bup(xx[e]) + bup(sv));
    }
    if (MODE != 0) st8(ws + O_HT + ((size_t)d * SL + tok) * 2, o[0]);
  }
  if (MODE == 0) {
    auto r0 = __builtin_amdgcn_permlane16_swap(o[0][0], o[1][0], false, false);
    auto r1 = __builtin_amdgcn_permlane16_swap(o[0][1], o[1][1], false, false);
    const int tok = 512 * w + 256 * (g & 1) + 16 * a + 8 * (g >> 1);
    st16(ws + O_HT + ((size_t)d * SL + tok) * 2, u32x4{r0[0], r1[0], r0[1], r1[1]});
  }
}

DEV void role_chan(int cb, const u16* __restrict__ lAr, const u16* __restrict__ Aim, const u16* __restrict__ Cre,
                   const u16* __restrict__ Cim, const u16* __restrict__ ldt, const u16* __restrict__ Dsk,
                   char* __restrict__ ws, char* sm STAMP_ARG) {
  const int tid = threadIdx.x, lane = tid & 63;
  const int w = __builtin_amdgcn_readfirstlane(tid >> 6);
  const int bx = cb & 7, bi = cb >> 3;
  const int mode = bi < 64 ? 0 : 1 + (bi & 1);
  const int ci = bi < 64 ? bi : 64 + ((bi - 64) >> 1);
  const int d = (ci >> 1) * 16 + bx * 2 + (ci & 1);
  const int n = tid & 127, part = w >> 1;
  const unsigned cr_b = Cre[d * NS + n], ci_b = Cim[d * NS + n];
  const unsigned cr_o = Cre[d * NS + (n ^ 64)], ci_o = Cim[d * NS + (n ^ 64)];
  const float lar = bf2f(lAr[d * NS + n]), aim = bf2f(Aim[d * NS + n]);
  const unsigned e0 = ld_agent(ws + O_SY);
  const float dsk = bf2f(Dsk[d]);
  const float dt = rbf(exp_acc(bf2f(ldt[d])));
  const float are = rbf(exp_acc(lar));
  const float ar2 = -are * dt * 1.4426950408889634f;
  const float ai = aim * dt;
  int mx = (int)max(max(cr_b & 0x7fffu, ci_b & 0x7fffu), max(cr_o & 0x7fffu, ci_o & 0x7fffu));
  mx = max(mx, __builtin_amdgcn_update_dpp(0, mx, 0xB1, 0xf, 0xf, true));
  mx = max(mx, __builtin_amdgcn_update_dpp(0, mx, 0x4E, 0xf, 0xf, true));
  mx = max(mx, __builtin_amdgcn_update_dpp(0, mx, 0x141, 0xf, 0xf, true));
  mx = max(mx, __builtin_amdgcn_update_dpp(0, mx, 0x140, 0xf, 0xf, true));
  const int mxs = max(max(__builtin_amdgcn_readlane(mx, 0), __builtin_amdgcn_readlane(mx, 16)),
                      max(__builtin_amdgcn_readlane(mx, 32), __builtin_amdgcn_readlane(mx, 48)));
  const unsigned ef = min(max((unsigned)mxs >> 7, 10u), 254u);
  const float sF = __uint_as_float((264u - ef) << 23);
  const float kinv = dt * __uint_as_float((ef - 6u) << 23);
  const float I2PH = 0.15915494309189535f;
  const float I2PL = (float)(0.15915494309189533577 - (double)0.15915494309189535f);
  const float chi = ai * I2PH;
  const float clo = fmaf(ai, I2PH, -chi) + ai * I2PL;
  auto anchor = [&](float p) -> cw {
    float m = __builtin_amdgcn_exp2f(__builtin_fabsf(p) * ar2);
    float rv = fmaf(p, clo, __builtin_amdgcn_fractf(p * chi));
    float c = __builtin_amdgcn_cosf(rv), sn = __builtin_amdgcn_sinf(rv);
    return cw{f32x2{c, sn} * bc2(m), f32x2{sn, c} * f32x2{-m, m}};
  };
  STAMP(1, 1);
  char* tw = sm + n * 64;
  const int sx = (n >> 1) & 3;
  auto put8 = [&](int tile, int half, const f32x2* v) {
    u32x4 re = {pkh2(f32x2{v[0].x, v[1].x}), pkh2(f32x2{v[2].x, v[3].x}), pkh2(f32x2{v[4].x, v[5].x}), pkh2(f32x2{v[6].x, v[7].x})};
    u32x4 im = {pkh2(f32x2{v[0].y, v[1].y}), pkh2(f32x2{v[2].y, v[3].y}), pkh2(f32x2{v[4].y, v[5].y}), pkh2(f32x2{v[6].y, v[7].y})};
    *(u32x4*)(tw + tile * 8192 + ((half ^ sx) << 4)) = re;
    *(u32x4*)(tw + tile * 8192 + (((2 + half) ^ sx) << 4)) = im;
  };
  const f32x2 C0 = f32x2{blo(cr_b) * sF, -blo(ci_b) * sF};
  if (part == 0) {
    f32x2 e[8], e2[8];
    cw w8 = anchor(8.f), w16 = csq(w8), w32 = anchor(32.f), w64 = csq(w32);
    e[0] = f32x2{1024.f, 0.f};
    e[1] = w8.v * bc2(1024.f);
    e[2] = w16.v * bc2(1024.f);
    e[3] = cmul(e[1], w16);
#pragma unroll
    for (int j = 0; j < 4; ++j) e[4 + j] = cmul(e[j], w32);
#pragma unroll
    for (int j = 0; j < 8; ++j) e2[j] = cmul(e[j], w64);
    put8(0, 0, e); put8(0, 1, e2);
    const cw w1 = anchor(-1.f), w2 = csq(w1), w4 = csq(w2), w128 = anchor(-128.f);
    e[0] = C0; e[1] = cmul(C0, w1); e[2] = cmul(C0, w2); e[3] = cmul(e[1], w2);
#pragma unroll
    for (int j = 0; j < 4; ++j) e[4 + j] = cmul(e[j], w4);
#pragma unroll
    for (int j = 0; j < 8; ++j) e2[j] = cmul(e[j], w128);
    put8(1, 0, e); put8(1, 1, e2);
  } else {
    const cw w2 = anchor(-2.f), w4 = csq(w2);
    const cw w128 = anchor(-128.f), w256 = csq(w128), w512 = anchor(-512.f), w1024 = csq(w512), w2048 = csq(w1024);
    f32x2 gq[8], r[8];
    gq[0] = C0; gq[1] = cmul(C0, w128); gq[2] = cmul(C0, w256); gq[3] = cmul(gq[1], w256);
#pragma unroll
    for (int j = 0; j < 4; ++j) gq[4 + j] = cmul(gq[j], w512);
#pragma unroll
    for (int I = 0; I < 2; ++I) {
      r[4 * I] = gq[2 + I]; r[4 * I + 1] = cmul(gq[2 + I], w2); r[4 * I + 2] = cmul(gq[2 + I], w4); r[4 * I + 3] = cmul(r[4 * I + 1], w4);
    }
    put8(2, 0, r);
#pragma unroll
    for (int I = 0; I < 4; ++I) { r[2 * I] = gq[4 + I]; r[2 * I + 1] = cmul(gq[4 + I], w4); }
    put8(2, 1, r);
#pragma unroll
    for (int j = 0; j < 8; ++j) r[j] = cmul(gq[j], w1024);
    put8(3, 0, r);
    r[0] = cmul(C0, w2048);
    put8(3, 1, r);
  }
  STAMP(1, 2);
  __syncthreads();
  _Float16* T = (_Float16*)(sm + KG_T);
  const unsigned tag = epoch_tag(e0);
  if (w < 3) {
    const int r = lane & 15, g = lane >> 4, q = r >> 2, p = r & 3;
    const int n0 = 4 * g + (q >> 1), ch = (q & 1) * 2 + (p >> 1);
    const unsigned a0 = n0 * 64 + ((ch ^ ((n0 >> 1) & 3)) << 4) + 8 * (p & 1);
    const unsigned a1 = (n0 + 2) * 64 + ((ch ^ (((n0 + 2) >> 1) & 3)) << 4) + 8 * (p & 1);
    const char* ta = sm + (w + 1) * 8192;
    f32x4 acc0 = {0.f, 0.f, 0.f, 0.f}, acc1 = {0.f, 0.f, 0.f, 0.f};
#pragma unroll
    for (int ks = 0; ks < 8; ++ks) {
      u32x2 x0 = tr_read(ta, a0 + ks * 1024), x1 = tr_read(ta, a1 + ks * 1024);
      u32x2 y0 = tr_read(sm, a0 + ks * 1024), y1 = tr_read(sm, a1 + ks * 1024);
      if (ks & 1) acc1 = mfma_h(u32x4{x0[0], x0[1], x1[0], x1[1]}, u32x4{y0[0], y0[1], y1[0], y1[1]}, acc1);
      else acc0 = mfma_h(u32x4{x0[0], x0[1], x1[0], x1[1]}, u32x4{y0[0], y0[1], y1[0], y1[1]}, acc0);
    }
    const f32x4 kk = (acc0 + acc1) * (1.0f / 8192.0f);
#pragma unroll
    for (int e = 0; e < 4; ++e) {
      const int rr = 4 * g + e;
      const int c0 = 128 * (rr >> 3) + (rr & 7) + 8 * r;
      const int c1 = rr < 8 ? 256 + 64 * (rr >> 2) + (rr & 3) + 4 * r : 384 + 32 * ((rr - 8) >> 1) + (rr & 1) + 2 * r;
      const int c2 = rr <= 8 ? 512 + 16 * rr + r : 680;
      T[w == 0 ? c0 : (w == 1 ? c1 : c2)] = (_Float16)kk[e];
    }
  } else {
    const char* rf = ws + O_SY + SY_R + (cb & 31) * 128;
    for (unsigned spin = 0; spin < (1u << 18); ++spin) {
      if (__builtin_amdgcn_readfirstlane(ld_agent(rf)) == tag) break;
      __builtin_amdgcn_s_sleep(4);
    }
    if (lane == 0) __hip_atomic_fetch_add((unsigned*)(ws + O_SY + SY_D + (cb & 15) * 128), 1u, __ATOMIC_RELAXED, __HIP_MEMORY_SCOPE_AGENT);
  }
  STAMP(1, 3);
  __syncthreads();
  STAMP(1, 4);
  u32x4 xu0, xu1;
  {
    const unsigned xo = (unsigned)(((tid >> 2) * DM + d) * 64 + (tid & 3) * 16);
    GLD16S(xu0, ws + O_XU, xo, 0);
    GLD16S(xu1, ws + O_XU + (size_t)64 * DM * 64, xo, 0);
  }
  {
    const u16* Tu = (const u16*)T;
    unsigned wv[6];
#pragma unroll
    for (int e2 = 0; e2 < 6; ++e2) {
      const int l0 = 2047 - (tid * 8 + 2 * e2), l1 = l0 - 1;
      const unsigned bb = pkbf((float)l0, (float)l1);
      int c0 = l0 < 128 ? l0 : (int)(bb & 0xffffu) - 17024;
      int c1 = l1 < 128 ? l1 : (int)(bb >> 16) - 17024;
      if (e2 >= 4) { c0 = max(c0, 0); c1 = max(c1, 0); }
      unsigned v = (unsigned)Tu[c0] | ((unsigned)Tu[c1] << 16);
      if (e2 >= 4 && l0 < 0) v = 0u;
      wv[e2] = v;
    }
    unsigned al[5];
#pragma unroll
    for (int i = 0; i < 5; ++i) al[i] = __builtin_amdgcn_alignbit(wv[i + 1], wv[i], 16);
#pragma unroll
    for (int c = 0; c < 2; ++c) {
      *(u32x4*)(sm + (2 * c) * KC_STR + tid * 16) = u32x4{wv[c], wv[c + 1], wv[c + 2], wv[c + 3]};
      *(u32x4*)(sm + (2 * c + 1) * KC_STR + tid * 16) = u32x4{al[c], al[c + 1], al[c + 2], al[c + 3]};
    }
    if (tid < 8) *(u32x4*)(sm + (tid >> 1) * KC_STR + 4096 + (tid & 1) * 16) = u32x4{0, 0, 0, 0};
    if (tid < 32) *(u32x4*)(sm + KC_U + tid * 16) = u32x4{0, 0, 0, 0};
  }
  STAMP(1, 5);
  asm volatile("s_waitcnt vmcnt(0)" : "+v"(xu0), "+v"(xu1)::"memory");
  {
    char* xd = sm + ((tid & 2) ? KC_U + 512 : KC_X) + (tid >> 2) * 32 + (tid & 1) * 16;
    *(u32x4*)(xd) = xu0;
    *(u32x4*)(xd + 2048) = xu1;
  }
  STAMP(1, 6);
  __syncthreads();
  STAMP(1, 7);
  if (mode == 0) conv_body<0>(d, w, lane, kinv, dsk, ws, sm STAMP_PASS);
  else if (mode == 1) conv_body<1>(d, w, lane, kinv, dsk, ws, sm STAMP_PASS);
  else conv_body<2>(d, w, lane, kinv, dsk, ws, sm STAMP_PASS);
}

__global__ __launch_bounds__(256, 4) void k_front(const u16* __restrict__ X, const u16* __restrict__ lAr,
                                                  const u16* __restrict__ Aim, const u16* __restrict__ Cre,
                                                  const u16* __restrict__ Cim, const u16* __restrict__ ldt,
                                                  const u16* __restrict__ Dsk, const u16* __restrict__ w1,
                                                  const u16* __restrict__ w2, const u16* __restrict__ w3,
                                                  const u16* __restrict__ w4, char* __restrict__ ws) {
  __shared__ __attribute__((aligned(16))) char sm[40960];
  const int b = blockIdx.x;
  STAMP_DECL; STAMP(0, 0);
  if (b >= 256) role_chan(b - 256, lAr, Aim, Cre, Cim, ldt, Dsk, ws, sm STAMP_PASS);
  else if (b < 128) role_ln(b, X, ws, sm STAMP_PASS);
  else if (b == 128) { if (threadIdx.x < 64) role_collect(ws); }
  else if (b < 129 + 93) {
    const int pb = b - 129;
    if (pb < 24) st16(ws + O_CNT + pb * 4096 + threadIdx.x * 16, u32x4{0, 0, 0, 0});
    role_pack(2 * pb, w1, w2, w3, w4, ws, sm);
    if (2 * pb + 1 < 185) role_pack(2 * pb + 1, w1, w2, w3, w4, ws, sm);
  } else {
    role_colsum(b - 222, w1, w3, ws, sm);
    if (b - 222 + 34 < 37) { __syncthreads(); role_colsum(b - 222 + 34, w1, w3, ws, sm); }
  }
  STAMP(0, 14);
  STAMP(0, 15);
  STAMP_FLUSH(0, ws);
}

DEV unsigned frag_off(int t, int c, int KS) {
  return (unsigned)((((t >> 4) * KS + (c >> 2)) * 64 + (c & 3) * 16 + (t & 15)) * 16);
}

DEV int tile5(int nb, int j) { return (nb & 1) ? (j < 4 ? 5 * nb + 1 + j : 5 * nb) : 5 * nb + j; }

#define W4SLOT(c) ((c) % 5 < 2 ? 102400 + ((c) % 5) * 30720 : ((c) % 5 - 2) * 30720)
#define BAR() asm volatile("s_barrier" ::: "memory")
#define VMBAR(N) asm volatile("s_waitcnt vmcnt(%0)\n\ts_barrier" ::"i"(N) : "memory")

#define PSLEEP 4
DEV void wait_counters(const char* cnt, int idx, bool active, unsigned) {
  const unsigned* p = (const unsigned*)(cnt + 49152 + (size_t)idx * 128);
  for (unsigned spin = 0; spin < (1u << 18); ++spin) {
    unsigned v = 1u;
    if (active) v = __hip_atomic_load(p, __ATOMIC_RELAXED, __HIP_MEMORY_SCOPE_AGENT);
    if (__all(v != 0u)) break;
    __builtin_amdgcn_s_sleep(PSLEEP);
  }
}
DEV void signal_counter(char* cnt, int idx, unsigned target) {
  const unsigned old = __hip_atomic_fetch_add((unsigned*)(cnt + (size_t)idx * 128), 1u, __ATOMIC_RELAXED, __HIP_MEMORY_SCOPE_AGENT);
  if (old == target - 1u) __hip_atomic_store((unsigned*)(cnt + 49152 + (size_t)idx * 128), 1u, __ATOMIC_RELAXED, __HIP_MEMORY_SCOPE_AGENT);
}

__global__ __launch_bounds__(768, 3) void k_chain(char* __restrict__ ws, u16* __restrict__ out) {
  __shared__ __attribute__((aligned(16))) char sm[163840];
  STAMP_DECL; STAMP(2, 0);
  const int tid = threadIdx.x, lane = tid & 63;
  const int w = __builtin_amdgcn_readfirstlane(tid >> 6);
  const int xcd = blockIdx.x & 7, jj = blockIdx.x >> 3;
  const int tt = xcd * 4 + (jj >> 3), nb = jj & 7;
  const int tb = (xcd >> 1) * 4 + (jj >> 3), nbk = (xcd & 1) * 8 + (jj & 7);
  const int g = lane >> 4, i = lane & 15;
  const int J0 = tile5(nb, 0) >> 1, J4 = tile5(nb, 4) >> 1, h4 = nb & 1;
  const int P3 = ((nbk >> 1) * 47) / 7, h6 = nbk & 1;
  const int K0p = P3 + 4 * h6, K6p = P3 + 3;
  const unsigned smb = lds_addr(sm);
  const unsigned vl = lane * 16;
  char* cnt = ws + O_CNT;

  if (w >= 8) {
    const int lw = w - 8;
    u32x4 keep = {0, 0, 0, 0};
    {
      const unsigned vo0 = (unsigned)(lane >> 3) * 4096u + (unsigned)(((lane & 7) ^ swz128(lane >> 3)) << 4);
      const unsigned vo1 = (unsigned)(lane >> 3) * 4096u + (unsigned)(((lane & 7) ^ swz128(8 + (lane >> 3))) << 4);
      const char* hsrc = ws + O_HT + (size_t)tt * 128 + (size_t)lw * 4 * 32768;
      const char* wA = ws + O_W1 + (size_t)tile5(nb, lw) * 20480;
      const char* wB = ws + O_W1 + (size_t)tile5(nb, 4) * 20480 + lw * 1024;
#define L_W5(base, c)                                                                        \
  do {                                                                                       \
    DMA16x4(smb + (unsigned)(base) + (unsigned)lw * 4096u, wA + (c) * 4096, vl);             \
    DMA16(smb + (unsigned)(base) + 16384u + (unsigned)lw * 1024u, wB + (c) * 4096, vl, 0);   \
  } while (0)
#define L_A1(c)                                                                              \
  _Pragma("unroll") for (int j_ = 0; j_ < 4; ++j_)                                           \
      DMA16(smb + 81920u + (unsigned)((c) * 16 + 4 * lw + j_) * 1024u, hsrc + (size_t)(16 * (c) + j_) * 32768, (j_ & 1) ? vo1 : vo0, 0)
      L_W5(0, 0); L_W5(20480, 1); L_A1(0); L_A1(1); L_A1(2);
      VMBAR(8);
      L_A1(3); L_W5(40960, 2);
      VMBAR(13);
      L_A1(4); L_W5(61440, 3);
      VMBAR(9);
      L_W5(0, 4);
      {
        const int un = jj * 4 + lw;
        const char* wp = ws + O_W2 + (size_t)un * 6400;
#pragma unroll
        for (int j = 0; j < 7; ++j)
          asm volatile("global_load_dwordx4 %0, %1, %2 offset:%3" : "+v"(keep) : "v"(vl), "s"(wp + (j >> 2) * 4096), "i"(j < 6 ? (j & 3) * 1024 : 1280) : "memory");
        const int u3 = un < 112 ? un : un - 16;
        const char* wq = ws + O_W3 + (size_t)(((xcd & 1) ? 52 : 0) + (u3 >> 1)) * 20480 + (u3 & 1) * 4096;
#pragma unroll
        for (int j = 0; j < 4; ++j)
          asm volatile("global_load_dwordx4 %0, %1, %2 offset:%3" : "+v"(keep) : "v"(vl), "s"(wq), "i"(j * 1024) : "memory");
      }
      VMBAR(16);
      VMBAR(11);
      BAR();
      wA += O_W2 - O_W1; wB += O_W2 - O_W1;
#pragma unroll
      for (int c = 0; c < 5; ++c) L_W5(c * 20480, c);
    }
    const char* w3a = ws + O_W3 + (size_t)(2 * K0p) * 20480 + lw * 1024;
    const char* w3b = ws + O_W3 + (size_t)(2 * K6p + h6) * 20480 + lw * 1024;
#define L_W7(base, c)                                                                                    \
  do {                                                                                                   \
    _Pragma("unroll") for (int j_ = 0; j_ < 6; ++j_)                                                     \
        DMA16(smb + (unsigned)(base) + (unsigned)(j_ * 4 + lw) * 1024u, w3a + j_ * 20480 + (c) * 4096, vl, 0); \
    DMA16(smb + (unsigned)(base) + (unsigned)(24 + lw) * 1024u, w3b + (c) * 4096, vl, 0);                \
  } while (0)
    L_W7(102400, 0); L_W7(102400 + 28672, 1);
    STAMP(3, 1);
    VMBAR(14);
    asm volatile("" ::"v"(keep));
    STAMP(3, 4);
    BAR();
    L_W7(0, 2); L_W7(28672, 3); L_W7(57344, 4);
    STAMP(3, 5);
    VMBAR(0);
    STAMP(3, 8);
    unsigned vo[8];
#pragma unroll
    for (int j = 0; j < 8; ++j) {
      const unsigned pos = (unsigned)lw * 7680u + j * 1024u + vl;
      const unsigned f = pos >> 10;
      const unsigned tl = (f * 43u) >> 8;
      const unsigned tg = tl < 4 ? (unsigned)(2 * J0) + tl : (unsigned)(2 * J4 + h4);
      vo[j] = tg * (54u * 1024u) + (f - tl * 6u) * 1024u + (pos & 1023u);
    }
    const char* w4s = ws + O_W4;
#define L_W4(c)                                                                                          \
  do {                                                                                                   \
    const unsigned lo_ = smb + (unsigned)W4SLOT(c) + (unsigned)lw * 7680u;                               \
    _Pragma("unroll") for (int j_ = 0; j_ < 7; ++j_) DMA16(lo_ + j_ * 1024u, w4s + (c) * 6144, vo[j_], 0); \
    DMA16H(lo_ + 7168u, w4s + (c) * 6144, vo[7]);                                                        \
  } while (0)
    BAR(); L_W4(0); L_W4(1);
    BAR(); L_W4(2);
    BAR(); L_W4(3); L_W4(4);
    STAMP(3, 9);
    VMBAR(0);
    STAMP(3, 12);
    BAR();
    BAR(); L_W4(5);
    BAR(); L_W4(6);
    BAR(); L_W4(7);
    VMBAR(16); L_W4(8);
    VMBAR(16);
    VMBAR(8);
    VMBAR(0);
    STAMP_FLUSH(3, ws);
    return;
  }

  const int t16 = tt * 4 + w;
  const unsigned v4 = (unsigned)(((2 * h4 + (g >> 1)) * 16 + i) * 16 + (g & 1) * 8);
  unsigned vm = vl + 61440, vh = vl + 122880;
  asm volatile("" : "+v"(vm), "+v"(vh));
#define LDW(off) (*(const u32x4*)(sm + ((off) < 61440 ? vl + (off) : ((off) < 122880 ? vm + ((off) - 61440) : vh + ((off) - 122880)))))

  if (w < 4) {
    const int qq = i >> 2, p = i & 3;
    f32x4 cs[5];
    GLD16(cs[0], ws + O_CS1 + J0 * 128, (unsigned)(g * 32), 0);
    GLD16(cs[1], ws + O_CS1 + J0 * 128, (unsigned)(g * 32), 16);
    GLD16(cs[2], ws + O_CS1 + J0 * 128, (unsigned)(g * 32), 128);
    GLD16(cs[3], ws + O_CS1 + J0 * 128, (unsigned)(g * 32), 144);
    GLD16(cs[4], ws + O_CS1 + J4 * 128 + h4 * 64, (unsigned)(g * 16), 0);
    f32x4 acc[5], sa = {0.f, 0.f, 0.f, 0.f}, sq = {0.f, 0.f, 0.f, 0.f};
#pragma unroll
    for (int j = 0; j < 5; ++j) acc[j] = f32x4{0.f, 0.f, 0.f, 0.f};
    const int r0 = 8 * g + qq;
    unsigned ta = 81920 + r0 * 128 + (((2 * w + (p >> 1)) ^ swz128(r0)) << 4) + 8 * (p & 1);
    unsigned tc = ta + 40960;
    asm volatile("" : "+v"(ta), "+v"(tc));
    const u32x4 ones = {0x3f803f80u, 0x3f803f80u, 0x3f803f80u, 0x3f803f80u};
    u32x4 wf[2][5], bf[2], hv[2];
    u32x2 hv4;
#define S1_LB(dst, ks)                                                                            \
  do {                                                                                            \
    const unsigned a_ = (ks) < 10 ? ta + (ks) * 4096 : tc + ((ks) - 10) * 4096;                   \
    u32x2 b0_ = tr_read(sm, a_), b1_ = tr_read(sm, a_ + 512);                                     \
    dst = u32x4{b0_[0], b0_[1], b1_[0], b1_[1]};                                                  \
  } while (0)
#define S1_LD(ks, j) LDW((((ks) >> 2) & 3) * 20480 + ((j) * 4 + ((ks) & 3)) * 1024)
    BAR();
    STAMP(2, 1);
    S1_LB(bf[0], 0);
#pragma unroll
    for (int j = 0; j < 5; ++j) wf[0][j] = S1_LD(0, j);
#pragma unroll
    for (int c = 0; c < 5; ++c)
#pragma unroll
    for (int k4 = 0; k4 < 4; ++k4) {
      const int ks = c * 4 + k4;
      if (k4 == 3 && c < 4) BAR();
#pragma unroll
      for (int j = 0; j < 7; ++j) {
        if (ks < 19 && j == 0) S1_LB(bf[(ks + 1) & 1], ks + 1);
        if (ks < 19 && j < 5) wf[(ks + 1) & 1][j] = S1_LD(ks + 1, j);
        if (j < 5) acc[j] = mfma_bf(wf[ks & 1][j], bf[ks & 1], acc[j]);
        if (j == 5) sa = mfma_bf(ones, bf[ks & 1], sa);
        if (j == 6) sq = mfma_bf(bf[ks & 1], bf[ks & 1], sq);
        SCHED_FENCE();
      }
    }
    {
      const unsigned a0 = ta + J0 * 4096;
      u32x2 b0 = tr_read(sm, a0), b1 = tr_read(sm, a0 + 512), b2 = tr_read(sm, a0 + 4096), b3 = tr_read(sm, a0 + 4608);
      hv[0] = u32x4{b0[0], b0[1], b1[0], b1[1]};
      hv[1] = u32x4{b2[0], b2[1], b3[0], b3[1]};
      const int rr = 16 * h4 + 4 * g + qq;
      hv4 = tr_read(sm, 81920 + J4 * 4096 + rr * 128 + (((2 * w + (p >> 1)) ^ swz128(rr)) << 4) + 8 * (p & 1));
    }
    asm volatile("s_waitcnt vmcnt(0) lgkmcnt(0)\n\ts_barrier"
                 : "+v"(cs[0]), "+v"(cs[1]), "+v"(cs[2]), "+v"(cs[3]), "+v"(cs[4]), "+v"(hv[0]), "+v"(hv[1]), "+v"(hv4)::"memory");
    STAMP(2, 2);
    float s2 = (p & 2) ? ((p & 1) ? sq[3] : sq[2]) : ((p & 1) ? sq[1] : sq[0]);
    s2 = sum_rows(g == qq ? s2 : 0.f);
    const float mean = sa[0] * (1.0f / DM);
    const float var = fmaxf(s2 * (1.0f / DM) - mean * mean, 0.f);
    float mb, sb;
    ln_scalars(mean, var, mb, sb);
    const float is = __builtin_amdgcn_rcpf(sb);
    float mb2, sb2;
    ln_scalars((mean - mb) * is, var * is * is, mb2, sb2);
    const float S = sb * sb2, R = __builtin_amdgcn_rcpf(S), mo = mb + mb2 * sb;
    const float c1 = GC1 * R, c2 = GC2 * R * R * R;
    const int t = t16 * 16 + i;
    {
      const int grp = nb >> 1;
      const int sh = (grp == 0) ? 1 : (grp == 1) ? -1 : (grp == 2) ? 2 : -2;
      const int td = t + sh, tsrc = t - sh;
      const bool okd = td >= 0 && td < SL, zf = tsrc < 0 || tsrc >= SL;
      auto act = [&](int j, int e) -> unsigned {
        return gelu2(fma2(bc2(-mo), f32x2{cs[j][e], cs[j][e + 1]}, f32x2{acc[j][e], acc[j][e + 1]}), c1, c2, S);
      };
      char* od = ws + O_HB + frag_off(td, g, 20);
      char* oz = ws + O_HB + frag_off(t, g, 20);
      const int o4 = J4 * 1024 + ((2 * h4 + (g >> 1)) - g) * 256 + (g & 1) * 8;
#pragma unroll
      for (int pr = 0; pr < 2; ++pr) {
        u32x4 o = {act(2 * pr, 0), act(2 * pr, 2), act(2 * pr + 1, 0), act(2 * pr + 1, 2)};
        if (okd) st16(od + (J0 + pr) * 1024, o);
      }
      u32x2 o = {act(4, 0), act(4, 2)};
      if (okd) st8(od + o4, o);
      if (zf) {
        st16(oz + J0 * 1024, u32x4{0, 0, 0, 0});
        st16(oz + (J0 + 1) * 1024, u32x4{0, 0, 0, 0});
        st8(oz + o4, u32x2{0, 0});
      }
    }
    STAMP(2, 3);
    asm volatile("s_waitcnt vmcnt(0)" ::: "memory");
    if (lane == 0) signal_counter(cnt, t16, 8u);
    STAMP(2, 4);

    const char* asrc = ws + O_HB + (size_t)t16 * 20 * 1024;
    u32x4 af[20], zv[2];
    u32x2 zv4;
    {
      auto zf = [&](unsigned h2) -> unsigned { return pkbf2(bup(pkbf2(bup(h2) - bc2(mb))) * bc2(is)); };
#pragma unroll
      for (int e = 0; e < 4; ++e) { zv[0][e] = zf(hv[0][e]); zv[1][e] = zf(hv[1][e]); }
      zv4 = u32x2{zf(hv4[0]), zf(hv4[1])};
      asm volatile("" : "+v"(zv[0]), "+v"(zv[1]), "+v"(zv4));
    }
#define S2_LD(ks, j) LDW(((ks) >> 2) * 20480 + ((j) * 4 + ((ks) & 3)) * 1024)
    wait_counters(cnt, t16 - 1 + (lane & 3), lane < 3 && (unsigned)(t16 - 1 + lane) < 128u, 8u);
    STAMP(2, 5);
#pragma unroll
    for (int k = 0; k < 20; ++k) GLD16S(af[k], asrc + (k >> 2) * 4096, vl, (k & 3) * 1024);
    BAR();
#pragma unroll
    for (int j = 0; j < 5; ++j) { acc[j] = f32x4{0.f, 0.f, 0.f, 0.f}; wf[0][j] = S2_LD(0, j); }
#pragma unroll
    for (int c = 0; c < 5; ++c)
#pragma unroll
    for (int k4 = 0; k4 < 4; ++k4) {
      const int ks = c * 4 + k4;
      asm volatile("s_waitcnt vmcnt(%1)" : "+v"(af[ks]) : "i"(19 - ks) : "memory");
#pragma unroll
      for (int j = 0; j < 5; ++j) {
        if (ks < 19) wf[(ks + 1) & 1][j] = S2_LD(ks + 1, j);
        acc[j] = mfma_bf(wf[ks & 1][j], af[ks], acc[j]);
        SCHED_FENCE();
      }
    }
    BAR();
    STAMP(2, 6);
    {
      char* xo = ws + O_X1 + (size_t)t16 * 20 * 1024;
      auto x1v = [&](unsigned z, float a0, float a1) -> unsigned { return pkbf2(bup(z) + bup(pkbf(a0, a1))); };
      u32x4 o[2];
#pragma unroll
      for (int pr = 0; pr < 2; ++pr) {
        o[pr] = u32x4{x1v(zv[pr][0], acc[2 * pr][0], acc[2 * pr][1]), x1v(zv[pr][1], acc[2 * pr][2], acc[2 * pr][3]),
                      x1v(zv[pr][2], acc[2 * pr + 1][0], acc[2 * pr + 1][1]), x1v(zv[pr][3], acc[2 * pr + 1][2], acc[2 * pr + 1][3])};
        st16(xo + (J0 + pr) * 1024 + vl, o[pr]);
      }
      const u32x2 o4 = {x1v(zv4[0], acc[4][0], acc[4][1]), x1v(zv4[1], acc[4][2], acc[4][3])};
      st8(xo + J4 * 1024 + v4, o4);
      float s1 = 0.f, s2b = 0.f, s1b = 0.f, s2c = 0.f;
#pragma unroll
      for (int e = 0; e < 4; ++e) {
        s1 = dot2bf(o[0][e], 0x3f803f80u, s1); s2b = dot2bf(o[0][e], o[0][e], s2b);
        s1b = dot2bf(o[1][e], 0x3f803f80u, s1b); s2c = dot2bf(o[1][e], o[1][e], s2c);
      }
      s1 = dot2bf(o4[0], 0x3f803f80u, s1); s2b = dot2bf(o4[0], o4[0], s2b);
      s1b = dot2bf(o4[1], 0x3f803f80u, s1b); s2c = dot2bf(o4[1], o4[1], s2c);
      s1 = sum_rows(s1 + s1b); s2b = sum_rows(s2b + s2c);
      if (g == 0) st8(ws + O_S2 + ((size_t)nb * SL + t) * 8, u32x2{__float_as_uint(s1), __float_as_uint(s2b)});
#pragma unroll
      for (int pr = 0; pr < 2; ++pr) {
        *(u32x4*)(sm + 86016 + (w * 4 + pr) * 1024 + vl) = o[pr];
        *(u32x4*)(sm + 86016 + (w * 4 + 2 + pr) * 1024 + vl) = hv[pr];
      }
      *(u32x4*)(sm + 159744 + w * 1024 + vl) = u32x4{o4[0], o4[1], hv4[0], hv4[1]};
    }
    STAMP(2, 7);
    asm volatile("s_waitcnt vmcnt(0)" ::: "memory");
    if (lane == 0) signal_counter(cnt, 128 + t16, 8u);
  } else {
    BAR(); BAR(); BAR(); BAR(); BAR();
    BAR(); BAR(); BAR();
  }

  u32x4 rx[2], rh[2], r4;
  {
    const int u16t = tb * 8 + w;
    f32x4 cs[7];
#pragma unroll
    for (int j = 0; j < 6; ++j) GLD16(cs[j], ws + O_CS3 + K0p * 128, (unsigned)(g * 32), (j >> 1) * 128 + (j & 1) * 16);
    GLD16(cs[6], ws + O_CS3 + K6p * 128 + h6 * 64, (unsigned)(g * 16), 0);
    const char* asrc = ws + O_X1 + (size_t)u16t * 20 * 1024;
    u32x4 af[20], wf[2][7];
    f32x2 pa, pb;
    f32x4 acc[7];
#define S3_LD(ks, j) LDW((((ks) >> 2) < 2 ? 102400 + ((ks) >> 2) * 28672 : (((ks) >> 2) - 2) * 28672) + ((j) * 4 + ((ks) & 3)) * 1024)
#define S3_A(k) GLD16S(af[k], asrc + ((k) >> 2) * 4096, vl, ((k) & 3) * 1024)
    if (w < 4) wait_counters(cnt, 128 + u16t + 4 * (lane & 1), lane < 2, 8u);
    else BAR();
    asm volatile("s_waitcnt vmcnt(0)"
                 : "+v"(cs[0]), "+v"(cs[1]), "+v"(cs[2]), "+v"(cs[3]), "+v"(cs[4]), "+v"(cs[5]), "+v"(cs[6])::"memory");
    STAMP(2, 8);
#pragma unroll
    for (int k = 0; k < 10; ++k) S3_A(k);
    if (w < 4) BAR();
    r4 = *(const u32x4*)(sm + 159744 + (w & 3) * 1024 + vl);
#pragma unroll
    for (int j = 0; j < 7; ++j) { acc[j] = f32x4{0.f, 0.f, 0.f, 0.f}; wf[0][j] = S3_LD(0, j); }
#pragma unroll
    for (int c = 0; c < 5; ++c)
#pragma unroll
    for (int k4 = 0; k4 < 4; ++k4) {
      const int ks = c * 4 + k4;
      if (ks == 9 || ks == 17) BAR();
      asm volatile("s_waitcnt vmcnt(%1)" : "+v"(af[ks]) : "i"(ks <= 10 ? 9 : 19 - ks + 2) : "memory");
#pragma unroll
      for (int j = 0; j < 7; ++j) {
        if (j == 1 && ks < 10) S3_A(ks + 10);
        if (j == 1 && ks == 10) {
          GLD8S(pa, ws + O_S2 + (size_t)u16t * 128, (unsigned)(2 * g * SL * 8 + i * 8));
          GLD8S(pb, ws + O_S2 + (size_t)u16t * 128 + SL * 8, (unsigned)(2 * g * SL * 8 + i * 8));
        }
        if (ks < 19) wf[(ks + 1) & 1][j] = S3_LD(ks + 1, j);
        acc[j] = mfma_bf(wf[ks & 1][j], af[ks], acc[j]);
        SCHED_FENCE();
      }
    }
    rx[0] = *(const u32x4*)(sm + 86016 + ((w & 3) * 4 + 0) * 1024 + vl);
    rx[1] = *(const u32x4*)(sm + 86016 + ((w & 3) * 4 + 1) * 1024 + vl);
    rh[0] = *(const u32x4*)(sm + 86016 + ((w & 3) * 4 + 2) * 1024 + vl);
    rh[1] = *(const u32x4*)(sm + 86016 + ((w & 3) * 4 + 3) * 1024 + vl);
    asm volatile("s_waitcnt vmcnt(0) lgkmcnt(0)\n\ts_barrier"
                 : "+v"(pa), "+v"(pb), "+v"(rx[0]), "+v"(rx[1]), "+v"(rh[0]), "+v"(rh[1]), "+v"(r4)::"memory");
    STAMP(2, 9);
    const float s1 = sum_rows(pa.x + pb.x), s2 = sum_rows(pa.y + pb.y);
    const float mean = s1 * (1.0f / DM);
    const float var = fmaxf(s2 * (1.0f / DM) - mean * mean, 0.f);
    float mb, sb;
    ln_scalars(mean, var, mb, sb);
    const float is = __builtin_amdgcn_rcpf(sb);
    const float c1 = GC1 * is, c2 = GC2 * is * is * is;
    char* ao = ws + O_A3 + (size_t)u16t * 54 * 1024 + vl;
    auto act = [&](int j, int e) -> unsigned {
      return gelu2(fma2(bc2(-mb), f32x2{cs[j][e], cs[j][e + 1]}, f32x2{acc[j][e], acc[j][e + 1]}), c1, c2, sb);
    };
#pragma unroll
    for (int pr = 0; pr < 3; ++pr)
      st16(ao + (K0p + pr) * 1024, u32x4{act(2 * pr, 0), act(2 * pr, 2), act(2 * pr + 1, 0), act(2 * pr + 1, 2)});
    st8(ao + K6p * 1024 + ((2 * h6 + (g >> 1)) - g) * 256 + (g & 1) * 8, u32x2{act(6, 0), act(6, 2)});
    STAMP(2, 10);
    asm volatile("s_waitcnt vmcnt(0)" ::: "memory");
    if (lane == 0) signal_counter(cnt, 256 + u16t, 16u);
  }
  if (w >= 4) {
    BAR();
    BAR(); BAR(); BAR(); BAR(); BAR(); BAR(); BAR(); BAR();
    return;
  }

  {
    const char* asrc = ws + O_A3 + (size_t)t16 * 54 * 1024;
    u32x4 af[54], wf[2][5];
    f32x4 acc[5];
#define S4_LD(ks, j) LDW(W4SLOT((ks) / 6) + ((j) * 6 + (ks) % 6) * 1024)
#define S4_A(k) GLD16S(af[k], asrc + ((k) >> 2) * 4096, vl, ((k) & 3) * 1024)
    wait_counters(cnt, 256 + t16, lane == 0, 16u);
    STAMP(2, 11);
#pragma unroll
    for (int k = 0; k < 16; ++k) S4_A(k);
    BAR();
#pragma unroll
    for (int j = 0; j < 5; ++j) { acc[j] = f32x4{0.f, 0.f, 0.f, 0.f}; wf[0][j] = S4_LD(0, j); }
#pragma unroll
    for (int c = 0; c < 9; ++c)
#pragma unroll
    for (int k6 = 0; k6 < 6; ++k6) {
      const int ks = c * 6 + k6;
      if (k6 == 5 && c < 8) BAR();
      asm volatile("s_waitcnt vmcnt(%1)" : "+v"(af[ks]) : "i"(ks <= 38 ? 15 : 53 - ks) : "memory");
#pragma unroll
      for (int j = 0; j < 5; ++j) {
        if (j == 1 && ks < 38) S4_A(ks + 16);
        if (ks < 53) wf[(ks + 1) & 1][j] = S4_LD(ks + 1, j);
        acc[j] = mfma_bf(wf[ks & 1][j], af[ks], acc[j]);
        SCHED_FENCE();
      }
    }
    STAMP(2, 12);
    auto fin = [&](unsigned h2, unsigned x2, float a0, float a1) -> unsigned {
      unsigned hs = pkbf(a0, a1);
      unsigned bo = pkbf2(bup(x2) + bup(hs));
      return pkbf2(bup(h2) + bup(bo));
    };
    char* orow = (char*)out + (size_t)(t16 * 16 + i) * (DM * 2) + g * 16;
#pragma unroll
    for (int pr = 0; pr < 2; ++pr) {
      u32x4 o = {fin(rh[pr][0], rx[pr][0], acc[2 * pr][0], acc[2 * pr][1]), fin(rh[pr][1], rx[pr][1], acc[2 * pr][2], acc[2 * pr][3]),
                 fin(rh[pr][2], rx[pr][2], acc[2 * pr + 1][0], acc[2 * pr + 1][1]),
                 fin(rh[pr][3], rx[pr][3], acc[2 * pr + 1][2], acc[2 * pr + 1][3])};
      st16(orow + (J0 + pr) * 64, o);
    }
    st8(orow + J4 * 64 + h4 * 32 - g * 8, u32x2{fin(r4[2], r4[0], acc[4][0], acc[4][1]), fin(r4[3], r4[1], acc[4][2], acc[4][3])});
    STAMP(2, 13);
    STAMP(2, 14);
    STAMP_FLUSH(2, ws);
  }
}

extern "C" void kernel_launch(void* const* d_in, const int* in_sizes, int n_in, void* d_out, int out_size, void* d_ws,
                              size_t ws_size, hipStream_t stream) {
  (void)in_sizes; (void)n_in; (void)out_size; (void)ws_size;
  const u16* X = (const u16*)d_in[0];
  char* ws = (char*)d_ws;
  k_front<<<1024, 256, 0, stream>>>(X, (const u16*)d_in[1], (const u16*)d_in[2], (const u16*)d_in[3], (const u16*)d_in[4],
                                    (const u16*)d_in[5], (const u16*)d_in[6], (const u16*)d_in[8], (const u16*)d_in[9],
                                    (const u16*)d_in[10], (const u16*)d_in[11], ws);
  k_chain<<<256, 768, 0, stream>>>(ws, (u16*)d_out);
}
```

```cpp
#include <hip/hip_runtime.h>
#include <hip/hip_bf16.h>
#include <stdint.h>

typedef unsigned short u16;
typedef __attribute__((ext_vector_type(4))) unsigned u32x4;
typedef __attribute__((ext_vector_type(2))) unsigned u32x2;
typedef __attribute__((ext_vector_type(4))) float f32x4;
typedef __attribute__((ext_vector_type(2))) float f32x2;
typedef __attribute__((ext_vector_type(8))) __bf16 bf16x8;
typedef __attribute__((ext_vector_type(2))) __bf16 bf16x2;
typedef __attribute__((ext_vector_type(8))) _Float16 f16x8;
typedef __attribute__((ext_vector_type(2))) _Float16 f16x2;
typedef __attribute__((ext_vector_type(4))) short s16x4;
#define LDSP __attribute__((address_space(3)))
#define DEV __device__ __forceinline__
#define SCHED_FENCE() __builtin_amdgcn_sched_barrier(0)

constexpr int SL = 2048, DM = 640, NS = 128, FF = 1728;

constexpr size_t al4k(size_t x) { return (x + 4095) & ~(size_t)4095; }
constexpr size_t ACT = (size_t)SL * DM * 2;
constexpr size_t O_XU = 0;
constexpr size_t O_HT = O_XU + al4k(2 * ACT);
constexpr size_t O_W1 = O_HT + al4k(ACT);
constexpr size_t O_W2 = O_W1 + al4k((size_t)DM * DM * 2);
constexpr size_t O_W3 = O_W2 + al4k((size_t)DM * DM * 2);
constexpr size_t O_W4 = O_W3 + al4k((size_t)DM * FF * 2);
constexpr size_t O_CS1 = O_W4 + al4k((size_t)DM * FF * 2);
constexpr size_t O_CS3 = O_CS1 + 4096;
constexpr size_t O_S2 = O_CS3 + 8192;
constexpr size_t O_CNT = O_S2 + 131072;
constexpr size_t O_SY = O_CNT + 49152;
constexpr size_t O_HB = O_SY + 16384;
constexpr size_t O_X1 = O_HB + al4k(ACT);
constexpr size_t O_A3 = O_X1 + al4k(ACT);
constexpr size_t O_END = O_A3 + al4k((size_t)SL * FF * 2);

DEV float bf2f(u16 v) { return __uint_as_float((unsigned)v << 16); }
DEV float blo(unsigned v) { return __uint_as_float(v << 16); }
DEV float bhi(unsigned v) { return __uint_as_float(v & 0xffff0000u); }
DEV f32x2 bup(unsigned v) { return f32x2{blo(v), bhi(v)}; }
DEV unsigned pkbf2(f32x2 f) { return __builtin_bit_cast(unsigned, __builtin_convertvector(f, bf16x2)); }
DEV unsigned pkbf(float a, float b) { return pkbf2(f32x2{a, b}); }
DEV unsigned pkh2(f32x2 f) { return __builtin_bit_cast(unsigned, __builtin_convertvector(f, f16x2)); }
DEV float rbf(float a) { return blo(pkbf(a, a)); }
DEV f32x2 fma2(f32x2 a, f32x2 b, f32x2 c) { return __builtin_elementwise_fma(a, b, c); }
DEV f32x2 bc2(float a) { return f32x2{a, a}; }
DEV unsigned gelu2(f32x2 y, float c1, float c2, float sb) {
  f32x2 t = y * fma2(y * y, bc2(c2), bc2(c1));
  f32x2 e = {__builtin_amdgcn_exp2f(t.x), __builtin_amdgcn_exp2f(t.y)};
  f32x2 d = fma2(e, bc2(sb), bc2(sb));
  f32x2 r = {__builtin_amdgcn_rcpf(d.x), __builtin_amdgcn_rcpf(d.y)};
  return pkbf2(y * r);
}
constexpr float GC1 = -2.0f * 0.7978845608028654f * 1.4426950408889634f;
constexpr float GC2 = GC1 * 0.044715f;

DEV u32x2 tr_read(const char* lds_base, unsigned off) {
  s16x4 v = __builtin_amdgcn_ds_read_tr16_b64_v4i16((LDSP s16x4*)(lds_base + off));
  return __builtin_bit_cast(u32x2, v);
}
DEV f32x4 mfma_bf(u32x4 a, u32x4 b, f32x4 c) {
  return __builtin_amdgcn_mfma_f32_16x16x32_bf16(__builtin_bit_cast(bf16x8, a), __builtin_bit_cast(bf16x8, b), c, 0, 0, 0);
}
DEV f32x4 mfma_h(u32x4 a, u32x4 b, f32x4 c) {
  return __builtin_amdgcn_mfma_f32_16x16x32_f16(__builtin_bit_cast(f16x8, a), __builtin_bit_cast(f16x8, b), c, 0, 0, 0);
}
DEV float dot2bf(unsigned a, unsigned b, float c) {
  return __builtin_amdgcn_fdot2_f32_bf16(__builtin_bit_cast(bf16x2, a), __builtin_bit_cast(bf16x2, b), c, false);
}
DEV unsigned lds_addr(const void* p) { return (unsigned)(size_t)(LDSP const char*)p; }
template <int CTRL> DEV float dppf(float v) {
  return __builtin_bit_cast(float, __builtin_amdgcn_update_dpp(0, __builtin_bit_cast(int, v), CTRL, 0xf, 0xf, true));
}
DEV float sum16(float s) {
  s += dppf<0xB1>(s); s += dppf<0x4E>(s); s += dppf<0x141>(s); s += dppf<0x140>(s);
  return s;
}
DEV float sum_rows(float s) {
  unsigned u = __float_as_uint(s);
  auto a = __builtin_amdgcn_permlane16_swap(u, u, false, false);
  s = __uint_as_float(a[0]) + __uint_as_float(a[1]);
  u = __float_as_uint(s);
  auto b = __builtin_amdgcn_permlane32_swap(u, u, false, false);
  return __uint_as_float(b[0]) + __uint_as_float(b[1]);
}
DEV void st16(void* p, u32x4 v) { asm volatile("global_store_dwordx4 %0, %1, off sc1\n\ts_nop 1" ::"v"(p), "v"(v) : "memory"); }
DEV void st8(void* p, u32x2 v) { asm volatile("global_store_dwordx2 %0, %1, off sc1" ::"v"(p), "v"(v) : "memory"); }
DEV void st4(void* p, unsigned v) { asm volatile("global_store_dword %0, %1, off sc1" ::"v"(p), "v"(v) : "memory"); }

#define DMA16(lds, g, v, OFF)                                                                          \
  do {                                                                                                 \
    unsigned keep_;                                                                                    \
    asm volatile("s_mov_b32 %0, m0\n\ts_mov_b32 m0, %1\n\ts_nop 0\n\t"                                 \
                 "global_load_lds_dwordx4 %2, %3 offset:%4\n\ts_mov_b32 m0, %0"                        \
                 : "=&s"(keep_) : "s"(lds), "v"(v), "s"(g), "i"(OFF) : "memory");                      \
  } while (0)
#define DMA16H(lds, g, v)                                                                              \
  do {                                                                                                 \
    unsigned keep_;                                                                                    \
    asm volatile("s_mov_b32 %0, m0\n\ts_mov_b32 m0, %1\n\ts_mov_b32 exec_hi, 0\n\t"                    \
                 "global_load_lds_dwordx4 %2, %3\n\ts_mov_b32 exec_hi, -1\n\ts_mov_b32 m0, %0"         \
                 : "=&s"(keep_) : "s"(lds), "v"(v), "s"(g) : "memory");                                \
  } while (0)
#define DMA16x4(lds, g, v)                                                                             \
  do {                                                                                                 \
    unsigned keep_;                                                                                    \
    asm volatile("s_mov_b32 %0, m0\n\ts_mov_b32 m0, %1\n\ts_nop 0\n\t"                                 \
                 "global_load_lds_dwordx4 %2, %3\n\tglobal_load_lds_dwordx4 %2, %3 offset:1024\n\t"    \
                 "global_load_lds_dwordx4 %2, %3 offset:2048\n\tglobal_load_lds_dwordx4 %2, %3 offset:3072\n\t" \
                 "s_mov_b32 m0, %0"                                                                    \
                 : "=&s"(keep_) : "s"(lds), "v"(v), "s"(g) : "memory");                                \
  } while (0)
#define GLD8S(dst, g, v) asm volatile("global_load_dwordx2 %0, %1, %2 sc1" : "=v"(dst) : "v"(v), "s"(g) : "memory")
#define GLD16S(dst, g, v, OFF) \
  asm volatile("global_load_dwordx4 %0, %1, %2 offset:%3 sc1" : "=v"(dst) : "v"(v), "s"(g), "i"(OFF) : "memory")
#define GLD8(dst, g, v) asm volatile("global_load_dwordx2 %0, %1, %2" : "=v"(dst) : "v"(v), "s"(g) : "memory")
#define GLD16(dst, g, v, OFF) \
  asm volatile("global_load_dwordx4 %0, %1, %2 offset:%3" : "=v"(dst) : "v"(v), "s"(g), "i"(OFF) : "memory")

DEV void ln_scalars(float mean, float var, float& mb, float& sb) {
  mb = rbf(mean);
  sb = rbf(__builtin_amdgcn_sqrtf(rbf(rbf(var) + 1e-5f)));
}

DEV unsigned ld_agent(const char* p) { return __hip_atomic_load((const unsigned*)p, __ATOMIC_RELAXED, __HIP_MEMORY_SCOPE_AGENT); }
DEV unsigned poll_word(const char* p) {
  unsigned v;
  asm volatile("global_atomic_add %0, %1, %2, off sc0\n\ts_waitcnt vmcnt(0)" : "=&v"(v) : "v"(p), "v"(0u) : "memory");
  return v;
}
DEV void st_agent(char* p, unsigned v) { __hip_atomic_store((unsigned*)p, v, __ATOMIC_RELAXED, __HIP_MEMORY_SCOPE_AGENT); }
DEV unsigned epoch_tag(unsigned e0) { return e0 * 0x9E3779B1u + 0x7F4A7C15u; }
constexpr size_t SY_A = 1024, SY_R = 2048, SY_D = SY_R + 32 * 128;

DEV void role_ln(int b, const u16* __restrict__ X, char* __restrict__ ws, char* sm) {
  const int tid = threadIdx.x;
  __builtin_amdgcn_s_setprio(3);
  const unsigned e0 = ld_agent(ws + O_SY);
  const int tile = b;
  const int t0 = tile * 16, r = tid >> 4, sub = tid & 15;
  const u32x4* src = (const u32x4*)(X + (size_t)(t0 + r) * DM);
  u32x4 v[5];
#pragma unroll
  for (int j = 0; j < 5; ++j) v[j] = src[j * 16 + sub];
  const int fr = ((r & 3) | ((r >> 3) << 2)) << 1;
#pragma unroll
  for (int j = 0; j < 5; ++j) *(u32x4*)(sm + r * 1280 + (((j * 16 + sub) ^ fr) << 4)) = v[j];
  const int lane = tid & 63, w = tid >> 6, g = lane >> 4, i = lane & 15, qq = i >> 2, p = i & 3;
  const int r0 = (g & 1) * 8 + qq;
  const int f0 = ((r0 & 3) | ((r0 >> 3) << 2)) << 1;
  const char* src2 = sm + r0 * 1280 + 8 * (p & 1);
  char* go = ws + O_XU + ((size_t)tile * DM + (g >> 1) * 16 + i) * 32 + (g & 1) * 16;
  __syncthreads();
#pragma unroll
  for (int it = 0; it < 5; ++it) {
    const int cb = it * 4 + w;
    const unsigned a0 = (unsigned)(((4 * cb + 2 * (g >> 1) + (p >> 1)) ^ f0) << 4);
    u32x2 x0 = tr_read(src2, a0), x1 = tr_read(src2, a0 + 5120);
    st16(go + cb * 1024 + ACT, u32x4{x0[0], x0[1], x1[0], x1[1]});
  }
  float s = 0.f;
#pragma unroll
  for (int j = 0; j < 5; ++j)
#pragma unroll
    for (int e = 0; e < 4; ++e) s = dot2bf(v[j][e], 0x3f803f80u, s);
  const float mean = sum16(s) * (1.0f / DM);
  f32x2 q2 = {0.f, 0.f};
  f32x2 xf[20];
#pragma unroll
  for (int j = 0; j < 5; ++j)
#pragma unroll
    for (int e = 0; e < 4; ++e) {
      xf[j * 4 + e] = bup(v[j][e]);
      f32x2 a = xf[j * 4 + e] - bc2(mean);
      q2 = fma2(a, a, q2);
    }
  const float var = sum16(q2.x + q2.y) * (1.0f / DM);
  float mb, sb;
  ln_scalars(mean, var, mb, sb);
  const float is = __builtin_amdgcn_rcpf(sb);
#pragma unroll
  for (int j = 0; j < 5; ++j) {
    u32x4 o;
#pragma unroll
    for (int e = 0; e < 4; ++e) {
      unsigned dd = pkbf2(xf[j * 4 + e] - bc2(mb));
      unsigned uu = pkbf2(bup(dd) * bc2(is));
      o[e] = pkh2(bup(uu));
    }
    *(u32x4*)(sm + 20480 + r * 1280 + (((j * 16 + sub) ^ fr) << 4)) = o;
  }
  __syncthreads();
#pragma unroll
  for (int it = 0; it < 5; ++it) {
    const int cb = it * 4 + w;
    const unsigned a0 = (unsigned)(((4 * cb + 2 * (g >> 1) + (p >> 1)) ^ f0) << 4) + 20480u;
    u32x2 x0 = tr_read(src2, a0), x1 = tr_read(src2, a0 + 5120);
    st16(go + cb * 1024, u32x4{x0[0], x0[1], x1[0], x1[1]});
  }
  asm volatile("s_waitcnt vmcnt(0)\n\ts_barrier" ::: "memory");
  if (tid == 0) st_agent(ws + O_SY + SY_A + b * 4, epoch_tag(e0));
}

DEV void role_collect(char* __restrict__ ws) {
  const int lane = threadIdx.x;
  char* sy = ws + O_SY;
  const unsigned tag = epoch_tag(ld_agent(sy));
  if (lane < 16) st_agent(sy + SY_D + lane * 128, 0u);
  asm volatile("s_waitcnt vmcnt(0)" ::: "memory");
  for (unsigned spin = 0; spin < (1u << 18); ++spin) {
    unsigned a, c;
    asm volatile("global_atomic_add %0, %2, %3, off sc0\n\tglobal_atomic_add %1, %2, %3, off offset:256 sc0\n\ts_waitcnt vmcnt(0)"
                 : "=&v"(a), "=&v"(c) : "v"(sy + SY_A + lane * 4), "v"(0u) : "memory");
    if (__all(a == tag && c == tag)) break;
    __builtin_amdgcn_s_sleep(2);
  }
  if (lane < 32) st_agent(sy + SY_R + lane * 128, tag);
  for (unsigned spin = 0; spin < (1u << 18); ++spin) {
    __builtin_amdgcn_s_sleep(32);
    const unsigned v = lane < 16 ? ld_agent(sy + SY_D + lane * 128) : 48u;
    if (__all(v >= 48u)) break;
  }
  if (lane == 0) st_agent(sy, tag);
}

struct cw { f32x2 v, s; };
DEV f32x2 cmul(f32x2 a, const cw& b) { return fma2(f32x2{a.y, a.y}, b.s, f32x2{a.x, a.x} * b.v); }
DEV cw mkw(f32x2 v) { return cw{v, f32x2{-v.y, v.x}}; }
DEV cw csq(const cw& a) { return mkw(cmul(a.v, a)); }
DEV float exp_acc(float x) {
  const float L2E = 1.4426950408889634f, L2EL = (float)(1.4426950408889634074 - (double)1.4426950408889634f);
  float hi = x * L2E;
  float lo = fmaf(x, L2E, -hi) + x * L2EL;
  float r = __builtin_amdgcn_exp2f(hi);
  return fmaf(r * lo, 0.6931471805599453f, r);
}

DEV int swz128(int row) { return (((row >> 1) & 1) | (((row >> 3) & 1) << 1)) << 1; }

constexpr unsigned long long SPLIT5 = 0x0000000000021084ull;
constexpr unsigned long long SPLIT7 = (1ull << 3) | (1ull << 9) | (1ull << 16) | (1ull << 23) | (1ull << 29) | (1ull << 36) | (1ull << 43) | (1ull << 50);
DEV void role_pack(int b, const u16* __restrict__ w1, const u16* __restrict__ w2, const u16* __restrict__ w3,
                   const u16* __restrict__ w4, char* __restrict__ ws, char* sm) {
  const int tid = threadIdx.x, lane = tid & 63, w = tid >> 6;
  char* my = sm + w * 8192;
  char* dst[2];
  int kst[2], perm[2];
  u32x4 v[2][4];
#pragma unroll
  for (int s = 0; s < 2; ++s) {
    int un = (b * 4 + w) * 2 + s;
    const u16* W; char* P; int N, KS, ns;
    const bool is3 = un >= 400 && un < 940;
    if (un < 200) { W = w1; P = ws + O_W1; N = DM; KS = 20; ns = 10; }
    else if (un < 400) { un -= 200; W = w2; P = ws + O_W2; N = DM; KS = 20; ns = 10; }
    else if (un < 940) { un -= 400; W = w3; P = ws + O_W3; N = FF; KS = 20; ns = 27; }
    else { un -= 940; W = w4; P = ws + O_W4; N = DM; KS = 54; ns = 10; }
    const int ks = un / ns, st = un - ks * ns;
    const u16* src = W + (size_t)(ks * 32) * N + st * 64;
    dst[s] = P + ((size_t)(st * 4) * KS + ks) * 1024;
    kst[s] = KS;
    perm[s] = (int)(((is3 ? SPLIT7 : SPLIT5) >> (2 * st)) & 3);
#pragma unroll
    for (int j = 0; j < 4; ++j)
      v[s][j] = *(const u32x4*)(src + (size_t)(j * 8 + (lane >> 3)) * N + (lane & 7) * 8);
  }
#pragma unroll
  for (int s = 0; s < 2; ++s)
#pragma unroll
    for (int j = 0; j < 4; ++j) {
      const int row = j * 8 + (lane >> 3);
      *(u32x4*)(my + s * 4096 + row * 128 + (((lane & 7) ^ swz128(row)) << 4)) = v[s][j];
    }
  const int g = lane >> 4, i = lane & 15, qq = i >> 2, p = i & 3;
  const int r0 = 8 * g + qq, sw = swz128(r0);
#pragma unroll
  for (int s = 0; s < 2; ++s)
#pragma unroll
    for (int nt = 0; nt < 4; ++nt) {
      const bool nat = (perm[s] >> (nt >> 1)) & 1;
      const int ch = nat ? 2 * nt + (p >> 1) : (nt >> 1) * 4 + p;
      const int hb = nat ? (p & 1) : (nt & 1);
      const unsigned a0 = s * 4096 + r0 * 128 + ((ch ^ sw) << 4) + 8 * hb;
      u32x2 a = tr_read(my, a0), c = tr_read(my, a0 + 512);
      st16(dst[s] + (size_t)nt * kst[s] * 1024 + lane * 16, u32x4{a[0], a[1], c[0], c[1]});
    }
}

DEV void role_colsum(int b, const u16* __restrict__ w1, const u16* __restrict__ w3, char* __restrict__ ws, char* sm) {
  const int tid = threadIdx.x;
  const u16* W; char* out; int N;
  if (b < 10) { W = w1 + b * 64; out = ws + O_CS1 + b * 256; N = DM; }
  else { W = w3 + (b - 10) * 64; out = ws + O_CS3 + (b - 10) * 256; N = FF; }
  const int r = tid >> 3, c = tid & 7;
  u32x4 v[20];
#pragma unroll
  for (int j = 0; j < 20; ++j) v[j] = *(const u32x4*)(W + (size_t)(j * 32 + r) * N + c * 8);
  f32x2 a[4];
#pragma unroll
  for (int e = 0; e < 4; ++e) a[e] = f32x2{0.f, 0.f};
#pragma unroll
  for (int j = 0; j < 20; ++j)
#pragma unroll
    for (int e = 0; e < 4; ++e) a[e] += bup(v[j][e]);
  float* red = (float*)sm;
#pragma unroll
  for (int e = 0; e < 4; ++e) { red[r * 65 + c * 8 + 2 * e] = a[e].x; red[r * 65 + c * 8 + 2 * e + 1] = a[e].y; }
  __syncthreads();
  if (tid < 64) {
    float s = 0.f;
#pragma unroll
    for (int j = 0; j < 32; ++j) s += red[j * 65 + tid];
    st4(out + tid * 4, __float_as_uint(s));
  }
}

constexpr int KC_STR = 4160, KC_U = 35328;
constexpr int KG_T = 33280;

template <int MODE> DEV bool cv_has(int i) { return MODE == 0 || ((MODE == 1) == (i == 0 || i == 3 || i == 4 || i == 7)); }
template <int MODE> DEV int cv_rank(int i) { return MODE == 1 ? (i == 0 ? 0 : i == 3 ? 1 : i == 4 ? 2 : 3) : (i == 1 ? 0 : i == 2 ? 1 : i == 5 ? 2 : 3); }
template <int MODE> DEV int cv_tile(int r) { return MODE == 1 ? (r == 0 ? 0 : r == 1 ? 3 : r == 2 ? 4 : 7) : (r == 0 ? 1 : r == 1 ? 2 : r == 2 ? 5 : 6); }

template <int MODE>
DEV void conv_body(int d, int w, int lane, float kinv, float dsk, char* __restrict__ ws, char* sm) {
  const int a = lane & 15, g = lane >> 4;
  const unsigned pa = lds_addr(sm) + ((3 - a) & 3) * KC_STR + (507 - (a >> 2) + 2 * g - 8 * w - 480) * 8;
  const unsigned pb = lds_addr(sm) + KC_U + (-128 - 32 * w + 16 * a + 8 * g + 240) * 2;
  u32x2 Al[16], Ah[16], xr[2];
  u32x4 B[16];
#pragma unroll
  for (int s = 0; s < (MODE == 0 ? 2 : 1); ++s) {
    const int ti = MODE == 0 ? 2 * w + s : cv_tile<MODE>(w);
    GLD8S(xr[s], ws + O_XU + ACT, (unsigned)(((16 * ti + a) * DM + d) * 32 + g * 8));
  }
#define CV_LA(k)                                                                                          \
  asm volatile("ds_read_b64 %0, %2 offset:%3\n\tds_read_b64 %1, %2 offset:%4"                             \
               : "=&v"(Al[k]), "=&v"(Ah[k]) : "v"(pa), "i"((15 - (k)) * 256), "i"((15 - (k)) * 256 + 8) : "memory")
#define CV_LB(i) asm volatile("ds_read_b128 %0, %1 offset:%2" : "=v"(B[i]) : "v"(pb), "i"(256 * (i)) : "memory")
  CV_LA(0);
#pragma unroll
  for (int i = 1; i < 16; i += 2) CV_LB(i);
  CV_LA(1);
#pragma unroll
  for (int i = 0; i < 16; i += 2) CV_LB(i);
  CV_LA(2);
  f32x4 acc[8];
  char* red = sm + w * 8192 + lane * 16;
#pragma unroll
  for (int i = 0; i < 8; ++i) acc[i] = f32x4{0.f, 0.f, 0.f, 0.f};
#pragma unroll
  for (int k = 0; k < 16; ++k) {
    if (k == 0)
      asm volatile("s_waitcnt lgkmcnt(12)" : "+v"(Al[0]), "+v"(Ah[0]), "+v"(B[1]), "+v"(B[3]), "+v"(B[5]), "+v"(B[7]), "+v"(B[9]),
                   "+v"(B[11]), "+v"(B[13]), "+v"(B[15])::"memory");
    else if (k == 1)
      asm volatile("s_waitcnt lgkmcnt(6)" : "+v"(Al[1]), "+v"(Ah[1]), "+v"(B[0]), "+v"(B[2]), "+v"(B[4]), "+v"(B[6]), "+v"(B[8]),
                   "+v"(B[10]), "+v"(B[12]), "+v"(B[14])::"memory");
    else {
      const int up = 2 * k + 2 < 15 ? 2 * k + 2 : 15, yn = 2 * (up - k);
      if (k <= 8) asm volatile("s_waitcnt lgkmcnt(%2)" : "+v"(Al[k]), "+v"(Ah[k]) : "i"(yn < 15 ? yn : 15) : "memory");
      else asm volatile("" : "+v"(Al[k]), "+v"(Ah[k]));
    }
    if (k == 8) {
      asm volatile("s_waitcnt lgkmcnt(0)\n\ts_barrier" ::: "memory");
#pragma unroll
      for (int i = 0; i < 4; ++i)
        if (cv_has<MODE>(i)) *(f32x4*)(red + i * 1024) = acc[i];
    }
    if (k > 8 && (k & 1) == 0 && cv_has<MODE>((k >> 1) - 1)) *(f32x4*)(red + ((k >> 1) - 1) * 1024) = acc[(k >> 1) - 1];
    const u32x4 A = {Al[k][0], Al[k][1], Ah[k][0], Ah[k][1]};
    if (2 * k + 3 < 16) CV_LA(2 * k + 3 < 16 ? 2 * k + 3 : 15);
    if (2 * k + 4 < 16) CV_LA(2 * k + 4 < 16 ? 2 * k + 4 : 15);
#pragma unroll
    for (int i = (k >> 1); i < 8; ++i)
      if (cv_has<MODE>(i)) acc[i] = mfma_h(A, B[2 * i - k + 1], acc[i]);
    SCHED_FENCE();
  }
  if (MODE == 0) asm volatile("s_waitcnt vmcnt(0)" : "+v"(xr[0]), "+v"(xr[1])::"memory");
  else asm volatile("s_waitcnt vmcnt(0)" : "+v"(xr[0])::"memory");
  if (cv_has<MODE>(7)) *(f32x4*)(red + 7 * 1024) = acc[7];
  __syncthreads();
  f32x4 y[2];
  u32x2 o[2];
#pragma unroll
  for (int s = 0; s < (MODE == 0 ? 2 : 1); ++s) {
    const int ti = MODE == 0 ? 2 * w + s : cv_tile<MODE>(w);
    const int tok = 256 * ti + 16 * a + 4 * g;
    const char* rp = sm + ti * 1024 + lane * 16;
    y[s] = (*(const f32x4*)(rp) + *(const f32x4*)(rp + 8192)) + (*(const f32x4*)(rp + 16384) + *(const f32x4*)(rp + 24576));
    const u32x2 uh = *(const u32x2*)(sm + KC_U + 512 + tok * 2);
    const u32x2 xx = xr[s];
#pragma unroll
    for (int e = 0; e < 2; ++e) {
      const unsigned uhe = uh[e];
      f16x2 u2 = __builtin_bit_cast(f16x2, uhe);
      unsigned yb = pkbf2(f32x2{y[s][2 * e], y[s][2 * e + 1]} * bc2(kinv));
      unsigned ud = pkbf2(f32x2{(float)u2[0], (float)u2[1]} * bc2(dsk));
      unsigned sv = pkbf2(bup(yb) + bup(ud));
      o[s][e] = pkbf2(bup(xx[e]) + bup(sv));
    }
    if (MODE != 0) st8(ws + O_HT + ((size_t)d * SL + tok) * 2, o[0]);
  }
  if (MODE == 0) {
    auto r0 = __builtin_amdgcn_permlane16_swap(o[0][0], o[1][0], false, false);
    auto r1 = __builtin_amdgcn_permlane16_swap(o[0][1], o[1][1], false, false);
    const int tok = 512 * w + 256 * (g & 1) + 16 * a + 8 * (g >> 1);
    st16(ws + O_HT + ((size_t)d * SL + tok) * 2, u32x4{r0[0], r1[0], r0[1], r1[1]});
  }
}

DEV void role_chan(int cb, const u16* __restrict__ lAr, const u16* __restrict__ Aim, const u16* __restrict__ Cre,
                   const u16* __restrict__ Cim, const u16* __restrict__ ldt, const u16* __restrict__ Dsk,
                   char* __restrict__ ws, char* sm, const char* code0, const char* code1) {
  const int tid = threadIdx.x, lane = tid & 63;
  const int w = __builtin_amdgcn_readfirstlane(tid >> 6);
  const int bx = cb & 7, bi = cb >> 3;
  const int mode = bi < 64 ? 0 : 1 + (bi & 1);
  const int ci = bi < 64 ? bi : 64 + ((bi - 64) >> 1);
  const int d = (ci >> 2) * 32 + bx * 4 + (ci & 3);
  const int n = tid & 127, part = w >> 1;
#define GLDU16(dst, g, v) asm volatile("global_load_ushort %0, %1, %2" : "=v"(dst) : "v"(v), "s"(g) : "memory")
  unsigned cr_b, ci_b, cr_o, ci_o, lar_b, aim_b, dsk_b, ldt_b, e0;
  GLDU16(lar_b, lAr + d * NS, (unsigned)(n * 2)); GLDU16(ldt_b, ldt + d, 0u); GLDU16(aim_b, Aim + d * NS, (unsigned)(n * 2));
  GLDU16(cr_b, Cre + d * NS, (unsigned)(n * 2)); GLDU16(ci_b, Cim + d * NS, (unsigned)(n * 2));
  GLDU16(cr_o, Cre + d * NS, (unsigned)((n ^ 64) * 2)); GLDU16(ci_o, Cim + d * NS, (unsigned)((n ^ 64) * 2));
  GLDU16(dsk_b, Dsk + d, 0u);
  asm volatile("global_load_dword %0, %1, %2 sc1" : "=v"(e0) : "v"(0u), "s"(ws + O_SY) : "memory");
  {
    const char* p = code0 + (size_t)bi * 1024;
    if (p + 1024 > code1) p = code0;
    DMA16(lds_addr(sm) + 39936u, p, (unsigned)(lane * 16), 0);
  }
  unsigned xa[12], ts[4], tra0, tra1;
#pragma unroll
  for (int e2 = 0; e2 < 6; ++e2) {
    const int l0 = 2047 - (tid * 8 + 2 * e2), l1 = l0 - 1;
    const unsigned bb = pkbf((float)l0, (float)l1);
    int ca = l0 < 128 ? l0 : (int)(bb & 0xffffu) - 17024;
    int cc = l1 < 128 ? l1 : (int)(bb >> 16) - 17024;
    if (e2 >= 4 && l0 < 0) { ca = 700; cc = 700; }
    xa[2 * e2] = KG_T + ca * 2; xa[2 * e2 + 1] = KG_T + cc * 2;
  }
  {
    const int r = lane & 15, g = lane >> 4, q = r >> 2, p = r & 3;
    const int n0 = 4 * g + (q >> 1), ch = (q & 1) * 2 + (p >> 1);
    tra0 = n0 * 64 + ((ch ^ ((n0 >> 1) & 3)) << 4) + 8 * (p & 1);
    tra1 = (n0 + 2) * 64 + ((ch ^ (((n0 + 2) >> 1) & 3)) << 4) + 8 * (p & 1);
#pragma unroll
    for (int e = 0; e < 4; ++e) {
      const int rr = 4 * g + e;
      const int c0 = 128 * (rr >> 3) + (rr & 7) + 8 * r;
      const int c1 = rr < 8 ? 256 + 64 * (rr >> 2) + (rr & 3) + 4 * r : 384 + 32 * ((rr - 8) >> 1) + (rr & 1) + 2 * r;
      const int c2 = rr <= 8 ? 512 + 16 * rr + r : 680;
      ts[e] = KG_T + (w == 0 ? c0 : (w == 1 ? c1 : c2)) * 2;
    }
  }
  if (tid == 0) *(unsigned*)(sm + KG_T + 1400) = 0u;
  asm volatile("" : "+v"(xa[0]), "+v"(xa[1]), "+v"(xa[2]), "+v"(xa[3]), "+v"(xa[4]), "+v"(xa[5]), "+v"(xa[6]), "+v"(xa[7]),
               "+v"(xa[8]), "+v"(xa[9]), "+v"(xa[10]), "+v"(xa[11]), "+v"(ts[0]), "+v"(ts[1]), "+v"(ts[2]), "+v"(ts[3]),
               "+v"(tra0), "+v"(tra1));
  asm volatile("s_waitcnt vmcnt(1)"
               : "+v"(cr_b), "+v"(ci_b), "+v"(cr_o), "+v"(ci_o), "+v"(lar_b), "+v"(aim_b), "+v"(dsk_b), "+v"(ldt_b), "+v"(e0)::"memory");
  const float lar = blo(lar_b), aim = blo(aim_b), dsk = blo(dsk_b);
  const float dt = rbf(exp_acc(blo(ldt_b)));
  const float are = rbf(exp_acc(lar));
  const float ar2 = -are * dt * 1.4426950408889634f;
  const float ai = aim * dt;
  int mx = (int)max(max(cr_b & 0x7fffu, ci_b & 0x7fffu), max(cr_o & 0x7fffu, ci_o & 0x7fffu));
  mx = max(mx, __builtin_amdgcn_update_dpp(0, mx, 0xB1, 0xf, 0xf, true));
  mx = max(mx, __builtin_amdgcn_update_dpp(0, mx, 0x4E, 0xf, 0xf, true));
  mx = max(mx, __builtin_amdgcn_update_dpp(0, mx, 0x141, 0xf, 0xf, true));
  mx = max(mx, __builtin_amdgcn_update_dpp(0, mx, 0x140, 0xf, 0xf, true));
  const int mxs = max(max(__builtin_amdgcn_readlane(mx, 0), __builtin_amdgcn_readlane(mx, 16)),
                      max(__builtin_amdgcn_readlane(mx, 32), __builtin_amdgcn_readlane(mx, 48)));
  const unsigned ef = min(max((unsigned)mxs >> 7, 10u), 254u);
  const float sF = __uint_as_float((264u - ef) << 23);
  const float kinv = dt * __uint_as_float((ef - 6u) << 23);
  const float I2PH = 0.15915494309189535f;
  const float I2PL = (float)(0.15915494309189533577 - (double)0.15915494309189535f);
  const float chi = ai * I2PH;
  const float clo = fmaf(ai, I2PH, -chi) + ai * I2PL;
  auto anchor = [&](float p) -> cw {
    float m = __builtin_amdgcn_exp2f(__builtin_fabsf(p) * ar2);
    float rv = fmaf(p, clo, __builtin_amdgcn_fractf(p * chi));
    float c = __builtin_amdgcn_cosf(rv), sn = __builtin_amdgcn_sinf(rv);
    return cw{f32x2{c, sn} * bc2(m), f32x2{sn, c} * f32x2{-m, m}};
  };
  char* tw = sm + n * 64;
  const int sx = (n >> 1) & 3;
  auto put8 = [&](int tile, int half, const f32x2* v) {
    u32x4 re = {pkh2(f32x2{v[0].x, v[1].x}), pkh2(f32x2{v[2].x, v[3].x}), pkh2(f32x2{v[4].x, v[5].x}), pkh2(f32x2{v[6].x, v[7].x})};
    u32x4 im = {pkh2(f32x2{v[0].y, v[1].y}), pkh2(f32x2{v[2].y, v[3].y}), pkh2(f32x2{v[4].y, v[5].y}), pkh2(f32x2{v[6].y, v[7].y})};
    *(u32x4*)(tw + tile * 8192 + ((half ^ sx) << 4)) = re;
    *(u32x4*)(tw + tile * 8192 + (((2 + half) ^ sx) << 4)) = im;
  };
  const f32x2 C0 = f32x2{blo(cr_b) * sF, -blo(ci_b) * sF};
  if (part == 0) {
    f32x2 e[8], e2[8];
    cw w8 = anchor(8.f), w16 = csq(w8), w32 = anchor(32.f), w64 = csq(w32);
    e[0] = f32x2{1024.f, 0.f};
    e[1] = w8.v * bc2(1024.f);
    e[2] = w16.v * bc2(1024.f);
    e[3] = cmul(e[1], w16);
#pragma unroll
    for (int j = 0; j < 4; ++j) e[4 + j] = cmul(e[j], w32);
#pragma unroll
    for (int j = 0; j < 8; ++j) e2[j] = cmul(e[j], w64);
    put8(0, 0, e); put8(0, 1, e2);
    const cw w1 = anchor(-1.f), w2 = csq(w1), w4 = csq(w2), w128 = anchor(-128.f);
    e[0] = C0; e[1] = cmul(C0, w1); e[2] = cmul(C0, w2); e[3] = cmul(e[1], w2);
#pragma unroll
    for (int j = 0; j < 4; ++j) e[4 + j] = cmul(e[j], w4);
#pragma unroll
    for (int j = 0; j < 8; ++j) e2[j] = cmul(e[j], w128);
    put8(1, 0, e); put8(1, 1, e2);
  } else {
    const cw w2 = anchor(-2.f), w4 = csq(w2);
    const cw w128 = anchor(-128.f), w256 = csq(w128), w512 = anchor(-512.f), w1024 = csq(w512), w2048 = csq(w1024);
    f32x2 gq[8], r[8];
    gq[0] = C0; gq[1] = cmul(C0, w128); gq[2] = cmul(C0, w256); gq[3] = cmul(gq[1], w256);
#pragma unroll
    for (int j = 0; j < 4; ++j) gq[4 + j] = cmul(gq[j], w512);
#pragma unroll
    for (int I = 0; I < 2; ++I) {
      r[4 * I] = gq[2 + I]; r[4 * I + 1] = cmul(gq[2 + I], w2); r[4 * I + 2] = cmul(gq[2 + I], w4); r[4 * I + 3] = cmul(r[4 * I + 1], w4);
    }
    put8(2, 0, r);
#pragma unroll
    for (int I = 0; I < 4; ++I) { r[2 * I] = gq[4 + I]; r[2 * I + 1] = cmul(gq[4 + I], w4); }
    put8(2, 1, r);
#pragma unroll
    for (int j = 0; j < 8; ++j) r[j] = cmul(gq[j], w1024);
    put8(3, 0, r);
    r[0] = cmul(C0, w2048);
    put8(3, 1, r);
  }
  __syncthreads();
  const unsigned tag = epoch_tag(e0);
  if (w < 3) {
    const unsigned a0 = tra0, a1 = tra1;
    const char* ta = sm + (w + 1) * 8192;
    f32x4 acc0 = {0.f, 0.f, 0.f, 0.f}, acc1 = {0.f, 0.f, 0.f, 0.f};
#pragma unroll
    for (int ks = 0; ks < 8; ++ks) {
      u32x2 x0 = tr_read(ta, a0 + ks * 1024), x1 = tr_read(ta, a1 + ks * 1024);
      u32x2 y0 = tr_read(sm, a0 + ks * 1024), y1 = tr_read(sm, a1 + ks * 1024);
      if (ks & 1) acc1 = mfma_h(u32x4{x0[0], x0[1], x1[0], x1[1]}, u32x4{y0[0], y0[1], y1[0], y1[1]}, acc1);
      else acc0 = mfma_h(u32x4{x0[0], x0[1], x1[0], x1[1]}, u32x4{y0[0], y0[1], y1[0], y1[1]}, acc0);
    }
    const f32x4 kk = (acc0 + acc1) * (1.0f / 8192.0f);
#pragma unroll
    for (int e = 0; e < 4; ++e) *(_Float16*)(sm + ts[e]) = (_Float16)kk[e];
  }
  __syncthreads();
  {
    unsigned wv[6];
#pragma unroll
    for (int e2 = 0; e2 < 6; ++e2)
      wv[e2] = (unsigned)*(const u16*)(sm + xa[2 * e2]) | ((unsigned)*(const u16*)(sm + xa[2 * e2 + 1]) << 16);
    unsigned al[5];
#pragma unroll
    for (int i = 0; i < 5; ++i) al[i] = __builtin_amdgcn_alignbit(wv[i + 1], wv[i], 16);
#pragma unroll
    for (int c = 0; c < 2; ++c) {
      *(u32x4*)(sm + (2 * c) * KC_STR + tid * 16) = u32x4{wv[c], wv[c + 1], wv[c + 2], wv[c + 3]};
      *(u32x4*)(sm + (2 * c + 1) * KC_STR + tid * 16) = u32x4{al[c], al[c + 1], al[c + 2], al[c + 3]};
    }
    if (tid < 8) *(u32x4*)(sm + (tid >> 1) * KC_STR + 4096 + (tid & 1) * 16) = u32x4{0, 0, 0, 0};
    if (tid < 32) *(u32x4*)(sm + KC_U + tid * 16) = u32x4{0, 0, 0, 0};
  }
  if (w == 3) {
    const char* rf = ws + O_SY + SY_R + (cb & 31) * 128;
    for (unsigned spin = 0; spin < (1u << 18); ++spin) {
      unsigned rv = 0;
      if (lane == 0) rv = poll_word(rf);
      if (__builtin_amdgcn_readfirstlane(rv) == tag) break;
      __builtin_amdgcn_s_sleep(4);
    }
  }
  asm volatile("s_barrier" ::: "memory");
  {
    u32x4 xu0;
    GLD16S(xu0, ws + O_XU, (unsigned)(((tid >> 1) * DM + d) * 32 + (tid & 1) * 16), 0);
    asm volatile("s_waitcnt vmcnt(0)" : "+v"(xu0)::"memory");
    *(u32x4*)(sm + KC_U + 512 + tid * 16) = xu0;
  }
  if (tid == 0) __hip_atomic_fetch_add((unsigned*)(ws + O_SY + SY_D + (cb & 15) * 128), 1u, __ATOMIC_RELAXED, __HIP_MEMORY_SCOPE_AGENT);
  __syncthreads();
  if (mode == 0) conv_body<0>(d, w, lane, kinv, dsk, ws, sm);
  else if (mode == 1) conv_body<1>(d, w, lane, kinv, dsk, ws, sm);
  else conv_body<2>(d, w, lane, kinv, dsk, ws, sm);
}

DEV unsigned frag_off(int t, int c, int KS) {
  return (unsigned)((((t >> 4) * KS + (c >> 2)) * 64 + (c & 3) * 16 + (t & 15)) * 16);
}

DEV int tile5(int nb, int j) { return (nb & 1) ? (j < 4 ? 5 * nb + 1 + j : 5 * nb) : 5 * nb + j; }

#define W4SLOT(c) ((c) % 5 < 2 ? 102400 + ((c) % 5) * 30720 : ((c) % 5 - 2) * 30720)
#define BAR() asm volatile("s_barrier" ::: "memory")
#define VMBAR(N) asm volatile("s_waitcnt vmcnt(%0)\n\ts_barrier" ::"i"(N) : "memory")

DEV void wait_count(char* cnt, int idx, bool active, unsigned target) {
  unsigned* p = (unsigned*)(cnt + (size_t)idx * 128);
  __builtin_amdgcn_s_sleep(4);
  for (unsigned spin = 0; spin < (1u << 18); ++spin) {
    unsigned v = target;
    if (active) {
      v = poll_word((const char*)p);
    }
    if (__all(v >= target)) break;
    __builtin_amdgcn_s_sleep(4);
  }
}
DEV void signal_count(char* cnt, int idx) {
  __hip_atomic_fetch_add((unsigned*)(cnt + (size_t)idx * 128), 1u, __ATOMIC_RELAXED, __HIP_MEMORY_SCOPE_AGENT);
}

__global__ __launch_bounds__(768, 3) void k_chain(char* __restrict__ ws, u16* __restrict__ out) {
  __shared__ __attribute__((aligned(16))) char sm[163840];
  const int tid = threadIdx.x, lane = tid & 63;
  const int w = __builtin_amdgcn_readfirstlane(tid >> 6);
  const int xcd = blockIdx.x & 7, jj = blockIdx.x >> 3;
  const int tt = xcd * 4 + (jj >> 3), nb = jj & 7;
  const int tb = (xcd >> 1) * 4 + (jj >> 3), nbk = (xcd & 1) * 8 + (jj & 7);
  const int g = lane >> 4, i = lane & 15;
  const int J0 = tile5(nb, 0) >> 1, J4 = tile5(nb, 4) >> 1, h4 = nb & 1;
  const int P3 = ((nbk >> 1) * 47) / 7, h6 = nbk & 1;
  const int K0p = P3 + 4 * h6, K6p = P3 + 3;
  const unsigned smb = lds_addr(sm);
  const unsigned vl = lane * 16;
  char* cnt = ws + O_CNT;

  if (w >= 8) {
    const int lw = w - 8;
    {
      const unsigned vo0 = (unsigned)(lane >> 3) * 4096u + (unsigned)(((lane & 7) ^ swz128(lane >> 3)) << 4);
      const unsigned vo1 = (unsigned)(lane >> 3) * 4096u + (unsigned)(((lane & 7) ^ swz128(8 + (lane >> 3))) << 4);
      const char* hsrc = ws + O_HT + (size_t)tt * 128 + (size_t)lw * 4 * 32768;
      const char* wA = ws + O_W1 + (size_t)tile5(nb, lw) * 20480;
      const char* wB = ws + O_W1 + (size_t)tile5(nb, 4) * 20480 + lw * 1024;
#define L_W5(base, c)                                                                        \
  do {                                                                                       \
    DMA16x4(smb + (unsigned)(base) + (unsigned)lw * 4096u, wA + (c) * 4096, vl);             \
    DMA16(smb + (unsigned)(base) + 16384u + (unsigned)lw * 1024u, wB + (c) * 4096, vl, 0);   \
  } while (0)
#define L_A1(c)                                                                              \
  _Pragma("unroll") for (int j_ = 0; j_ < 4; ++j_)                                           \
      DMA16(smb + 81920u + (unsigned)((c) * 16 + 4 * lw + j_) * 1024u, hsrc + (size_t)(16 * (c) + j_) * 32768, (j_ & 1) ? vo1 : vo0, 0)
      L_A1(0); L_W5(0, 0); L_A1(1); L_W5(20480, 1); L_A1(2);
      VMBAR(13);
      L_A1(3); L_W5(40960, 2);
      VMBAR(13);
      L_A1(4); L_W5(61440, 3);
      VMBAR(9);
      L_W5(0, 4);
      VMBAR(5);
      VMBAR(0);
      BAR();
      wA += O_W2 - O_W1; wB += O_W2 - O_W1;
#pragma unroll
      for (int c = 0; c < 5; ++c) L_W5(c * 20480, c);
    }
    const char* w3a = ws + O_W3 + (size_t)(2 * K0p) * 20480 + lw * 1024;
    const char* w3b = ws + O_W3 + (size_t)(2 * K6p + h6) * 20480 + lw * 1024;
#define L_W7(base, c)                                                                                    \
  do {                                                                                                   \
    _Pragma("unroll") for (int j_ = 0; j_ < 6; ++j_)                                                     \
        DMA16(smb + (unsigned)(base) + (unsigned)(j_ * 4 + lw) * 1024u, w3a + j_ * 20480 + (c) * 4096, vl, 0); \
    DMA16(smb + (unsigned)(base) + (unsigned)(24 + lw) * 1024u, w3b + (c) * 4096, vl, 0);                \
  } while (0)
    L_W7(102400, 0); L_W7(102400 + 28672, 1);
    VMBAR(14);
    BAR();
    L_W7(0, 2); L_W7(28672, 3); L_W7(57344, 4);
    VMBAR(0);
    unsigned vo[8];
#pragma unroll
    for (int j = 0; j < 8; ++j) {
      const unsigned pos = (unsigned)lw * 7680u + j * 1024u + vl;
      const unsigned f = pos >> 10;
      const unsigned tl = (f * 43u) >> 8;
      const unsigned tg = tl < 4 ? (unsigned)(2 * J0) + tl : (unsigned)(2 * J4 + h4);
      vo[j] = tg * (54u * 1024u) + (f - tl * 6u) * 1024u + (pos & 1023u);
    }
    const char* w4s = ws + O_W4;
#define L_W4(c)                                                                                          \
  do {                                                                                                   \
    const unsigned lo_ = smb + (unsigned)W4SLOT(c) + (unsigned)lw * 7680u;                               \
    _Pragma("unroll") for (int j_ = 0; j_ < 7; ++j_) DMA16(lo_ + j_ * 1024u, w4s + (c) * 6144, vo[j_], 0); \
    DMA16H(lo_ + 7168u, w4s + (c) * 6144, vo[7]);                                                        \
  } while (0)
    BAR(); L_W4(0); L_W4(1);
    BAR(); L_W4(2);
    BAR(); L_W4(3); L_W4(4);
    VMBAR(0);
    BAR();
    BAR(); L_W4(5);
    BAR(); L_W4(6);
    BAR(); L_W4(7);
    VMBAR(16); L_W4(8);
    VMBAR(16);
    VMBAR(8);
    VMBAR(0);
    return;
  }

  const int t16 = tt * 4 + w;
  const unsigned v4 = (unsigned)(((2 * h4 + (g >> 1)) * 16 + i) * 16 + (g & 1) * 8);
  unsigned vm = vl + 61440, vh = vl + 122880;
  asm volatile("" : "+v"(vm), "+v"(vh));
#define LDW(off) (*(const u32x4*)(sm + ((off) < 61440 ? vl + (off) : ((off) < 122880 ? vm + ((off) - 61440) : vh + ((off) - 122880)))))

  if (w < 4) {
    const int qq = i >> 2, p = i & 3;
    f32x4 cs[5];
    f32x4 acc[5], sa = {0.f, 0.f, 0.f, 0.f}, sq = {0.f, 0.f, 0.f, 0.f};
#pragma unroll
    for (int j = 0; j < 5; ++j) acc[j] = f32x4{0.f, 0.f, 0.f, 0.f};
    const int r0 = 8 * g + qq;
    unsigned ta = 81920 + r0 * 128 + (((2 * w + (p >> 1)) ^ swz128(r0)) << 4) + 8 * (p & 1);
    unsigned tc = ta + 40960;
    asm volatile("" : "+v"(ta), "+v"(tc));
    const u32x4 ones = {0x3f803f80u, 0x3f803f80u, 0x3f803f80u, 0x3f803f80u};
    u32x4 wf[2][5], bf[2], hv[2];
    u32x2 hv4;
#define S1_LB(dst, ks)                                                                            \
  do {                                                                                            \
    const unsigned a_ = (ks) < 10 ? ta + (ks) * 4096 : tc + ((ks) - 10) * 4096;                   \
    u32x2 b0_ = tr_read(sm, a_), b1_ = tr_read(sm, a_ + 512);                                     \
    dst = u32x4{b0_[0], b0_[1], b1_[0], b1_[1]};                                                  \
  } while (0)
#define S1_LD(ks, j) LDW((((ks) >> 2) & 3) * 20480 + ((j) * 4 + ((ks) & 3)) * 1024)
    BAR();
    S1_LB(bf[0], 0);
#pragma unroll
    for (int j = 0; j < 5; ++j) wf[0][j] = S1_LD(0, j);
#pragma unroll
    for (int c = 0; c < 5; ++c)
#pragma unroll
    for (int k4 = 0; k4 < 4; ++k4) {
      const int ks = c * 4 + k4;
      if (k4 == 3 && c < 4) BAR();
      if (ks == 8) {
        GLD16(cs[0], ws + O_CS1 + J0 * 128, (unsigned)(g * 32), 0);
        GLD16(cs[1], ws + O_CS1 + J0 * 128, (unsigned)(g * 32), 16);
        GLD16(cs[2], ws + O_CS1 + J0 * 128, (unsigned)(g * 32), 128);
        GLD16(cs[3], ws + O_CS1 + J0 * 128, (unsigned)(g * 32), 144);
        GLD16(cs[4], ws + O_CS1 + J4 * 128 + h4 * 64, (unsigned)(g * 16), 0);
      }
#pragma unroll
      for (int j = 0; j < 7; ++j) {
        if (ks < 19 && j == 0) S1_LB(bf[(ks + 1) & 1], ks + 1);
        if (ks < 19 && j < 5) wf[(ks + 1) & 1][j] = S1_LD(ks + 1, j);
        if (j < 5) acc[j] = mfma_bf(wf[ks & 1][j], bf[ks & 1], acc[j]);
        if (j == 5) sa = mfma_bf(ones, bf[ks & 1], sa);
        if (j == 6) sq = mfma_bf(bf[ks & 1], bf[ks & 1], sq);
        SCHED_FENCE();
      }
    }
    {
      const unsigned a0 = ta + J0 * 4096;
      u32x2 b0 = tr_read(sm, a0), b1 = tr_read(sm, a0 + 512), b2 = tr_read(sm, a0 + 4096), b3 = tr_read(sm, a0 + 4608);
      hv[0] = u32x4{b0[0], b0[1], b1[0], b1[1]};
      hv[1] = u32x4{b2[0], b2[1], b3[0], b3[1]};
      const int rr = 16 * h4 + 4 * g + qq;
      hv4 = tr_read(sm, 81920 + J4 * 4096 + rr * 128 + (((2 * w + (p >> 1)) ^ swz128(rr)) << 4) + 8 * (p & 1));
    }
    asm volatile("s_waitcnt vmcnt(0) lgkmcnt(0)\n\ts_barrier"
                 : "+v"(cs[0]), "+v"(cs[1]), "+v"(cs[2]), "+v"(cs[3]), "+v"(cs[4]), "+v"(hv[0]), "+v"(hv[1]), "+v"(hv4)::"memory");
    float s2 = (p & 2) ? ((p & 1) ? sq[3] : sq[2]) : ((p & 1) ? sq[1] : sq[0]);
    s2 = sum_rows(g == qq ? s2 : 0.f);
    const float mean = sa[0] * (1.0f / DM);
    const float var = fmaxf(s2 * (1.0f / DM) - mean * mean, 0.f);
    float mb, sb;
    ln_scalars(mean, var, mb, sb);
    const float is = __builtin_amdgcn_rcpf(sb);
    float mb2, sb2;
    ln_scalars((mean - mb) * is, var * is * is, mb2, sb2);
    const float S = sb * sb2, R = __builtin_amdgcn_rcpf(S), mo = mb + mb2 * sb;
    const float c1 = GC1 * R, c2 = GC2 * R * R * R;
    const int t = t16 * 16 + i;
    {
      const int grp = nb >> 1;
      const int sh = (grp == 0) ? 1 : (grp == 1) ? -1 : (grp == 2) ? 2 : -2;
      const int td = t + sh, tsrc = t - sh;
      const bool okd = td >= 0 && td < SL, zf = tsrc < 0 || tsrc >= SL;
      auto act = [&](int j, int e) -> unsigned {
        return gelu2(fma2(bc2(-mo), f32x2{cs[j][e], cs[j][e + 1]}, f32x2{acc[j][e], acc[j][e + 1]}), c1, c2, S);
      };
      char* od = ws + O_HB + frag_off(td, g, 20);
      char* oz = ws + O_HB + frag_off(t, g, 20);
      const int o4 = J4 * 1024 + ((2 * h4 + (g >> 1)) - g) * 256 + (g & 1) * 8;
#pragma unroll
      for (int pr = 0; pr < 2; ++pr) {
        u32x4 o = {act(2 * pr, 0), act(2 * pr, 2), act(2 * pr + 1, 0), act(2 * pr + 1, 2)};
        if (okd) st16(od + (J0 + pr) * 1024, o);
      }
      u32x2 o = {act(4, 0), act(4, 2)};
      if (okd) st8(od + o4, o);
      if (zf) {
        st16(oz + J0 * 1024, u32x4{0, 0, 0, 0});
        st16(oz + (J0 + 1) * 1024, u32x4{0, 0, 0, 0});
        st8(oz + o4, u32x2{0, 0});
      }
    }
    asm volatile("s_waitcnt vmcnt(0)" ::: "memory");
    if (lane == 0) signal_count(cnt, t16);

    const char* asrc = ws + O_HB + (size_t)t16 * 20 * 1024;
    u32x4 af[20], zv[2];
    u32x2 zv4;
    {
      auto zf = [&](unsigned h2) -> unsigned { return pkbf2(bup(pkbf2(bup(h2) - bc2(mb))) * bc2(is)); };
#pragma unroll
      for (int e = 0; e < 4; ++e) { zv[0][e] = zf(hv[0][e]); zv[1][e] = zf(hv[1][e]); }
      zv4 = u32x2{zf(hv4[0]), zf(hv4[1])};
      asm volatile("" : "+v"(zv[0]), "+v"(zv[1]), "+v"(zv4));
    }
#define S2_LD(ks, j) LDW(((ks) >> 2) * 20480 + ((j) * 4 + ((ks) & 3)) * 1024)
    wait_count(cnt, t16 - 1 + (lane & 3), lane < 3 && (unsigned)(t16 - 1 + lane) < 128u, 8u);
#pragma unroll
    for (int k = 0; k < 20; ++k) GLD16S(af[k], asrc + (k >> 2) * 4096, vl, (k & 3) * 1024);
    BAR();
#pragma unroll
    for (int j = 0; j < 5; ++j) { acc[j] = f32x4{0.f, 0.f, 0.f, 0.f}; wf[0][j] = S2_LD(0, j); }
#pragma unroll
    for (int c = 0; c < 5; ++c)
#pragma unroll
    for (int k4 = 0; k4 < 4; ++k4) {
      const int ks = c * 4 + k4;
      asm volatile("s_waitcnt vmcnt(%1)" : "+v"(af[ks]) : "i"(19 - ks) : "memory");
#pragma unroll
      for (int j = 0; j < 5; ++j) {
        if (ks < 19) wf[(ks + 1) & 1][j] = S2_LD(ks + 1, j);
        acc[j] = mfma_bf(wf[ks & 1][j], af[ks], acc[j]);
        SCHED_FENCE();
      }
    }
    BAR();
    {
      char* xo = ws + O_X1 + (size_t)t16 * 20 * 1024;
      auto x1v = [&](unsigned z, float a0, float a1) -> unsigned { return pkbf2(bup(z) + bup(pkbf(a0, a1))); };
      u32x4 o[2];
#pragma unroll
      for (int pr = 0; pr < 2; ++pr) {
        o[pr] = u32x4{x1v(zv[pr][0], acc[2 * pr][0], acc[2 * pr][1]), x1v(zv[pr][1], acc[2 * pr][2], acc[2 * pr][3]),
                      x1v(zv[pr][2], acc[2 * pr + 1][0], acc[2 * pr + 1][1]), x1v(zv[pr][3], acc[2 * pr + 1][2], acc[2 * pr + 1][3])};
        st16(xo + (J0 + pr) * 1024 + vl, o[pr]);
      }
      const u32x2 o4 = {x1v(zv4[0], acc[4][0], acc[4][1]), x1v(zv4[1], acc[4][2], acc[4][3])};
      st8(xo + J4 * 1024 + v4, o4);
      float s1 = 0.f, s2b = 0.f, s1b = 0.f, s2c = 0.f;
#pragma unroll
      for (int e = 0; e < 4; ++e) {
        s1 = dot2bf(o[0][e], 0x3f803f80u, s1); s2b = dot2bf(o[0][e], o[0][e], s2b);
        s1b = dot2bf(o[1][e], 0x3f803f80u, s1b); s2c = dot2bf(o[1][e], o[1][e], s2c);
      }
      s1 = dot2bf(o4[0], 0x3f803f80u, s1); s2b = dot2bf(o4[0], o4[0], s2b);
      s1b = dot2bf(o4[1], 0x3f803f80u, s1b); s2c = dot2bf(o4[1], o4[1], s2c);
      s1 = sum_rows(s1 + s1b); s2b = sum_rows(s2b + s2c);
      if (g == 0) st8(ws + O_S2 + ((size_t)nb * SL + t) * 8, u32x2{__float_as_uint(s1), __float_as_uint(s2b)});
#pragma unroll
      for (int pr = 0; pr < 2; ++pr) {
        *(u32x4*)(sm + 86016 + (w * 4 + pr) * 1024 + vl) = o[pr];
        *(u32x4*)(sm + 86016 + (w * 4 + 2 + pr) * 1024 + vl) = hv[pr];
      }
      *(u32x4*)(sm + 159744 + w * 1024 + vl) = u32x4{o4[0], o4[1], hv4[0], hv4[1]};
    }
    asm volatile("s_waitcnt vmcnt(0)" ::: "memory");
    if (lane == 0) signal_count(cnt, 128 + t16);
  } else {
    BAR(); BAR(); BAR(); BAR(); BAR();
    BAR(); BAR(); BAR();
  }

  u32x4 rx[2], rh[2], r4;
  {
    const int u16t = tb * 8 + w;
    f32x4 cs[7];
    const char* asrc = ws + O_X1 + (size_t)u16t * 20 * 1024;
    u32x4 af[20], wf[2][7];
    f32x2 pa, pb;
    f32x4 acc[7];
#define S3_LD(ks, j) LDW((((ks) >> 2) < 2 ? 102400 + ((ks) >> 2) * 28672 : (((ks) >> 2) - 2) * 28672) + ((j) * 4 + ((ks) & 3)) * 1024)
#define S3_A(k) GLD16S(af[k], asrc + ((k) >> 2) * 4096, vl, ((k) & 3) * 1024)
    if (w < 4) wait_count(cnt, 128 + u16t + 4 * (lane & 1), lane < 2, 8u);
    else BAR();
#pragma unroll
    for (int k = 0; k < 10; ++k) S3_A(k);
    if (w < 4) BAR();
    r4 = *(const u32x4*)(sm + 159744 + (w & 3) * 1024 + vl);
#pragma unroll
    for (int j = 0; j < 7; ++j) { acc[j] = f32x4{0.f, 0.f, 0.f, 0.f}; wf[0][j] = S3_LD(0, j); }
#pragma unroll
    for (int c = 0; c < 5; ++c)
#pragma unroll
    for (int k4 = 0; k4 < 4; ++k4) {
      const int ks = c * 4 + k4;
      if (ks == 9 || ks == 17) BAR();
      asm volatile("s_waitcnt vmcnt(%1)" : "+v"(af[ks]) : "i"(ks <= 10 ? 9 : 19 - ks + 9) : "memory");
#pragma unroll
      for (int j = 0; j < 7; ++j) {
        if (j == 1 && ks < 10) S3_A(ks + 10);
        if (j == 1 && ks == 10) {
          GLD8S(pa, ws + O_S2 + (size_t)u16t * 128, (unsigned)(2 * g * SL * 8 + i * 8));
          GLD8S(pb, ws + O_S2 + (size_t)u16t * 128 + SL * 8, (unsigned)(2 * g * SL * 8 + i * 8));
#pragma unroll
          for (int q = 0; q < 6; ++q) GLD16(cs[q], ws + O_CS3 + K0p * 128, (unsigned)(g * 32), (q >> 1) * 128 + (q & 1) * 16);
          GLD16(cs[6], ws + O_CS3 + K6p * 128 + h6 * 64, (unsigned)(g * 16), 0);
        }
        if (ks < 19) wf[(ks + 1) & 1][j] = S3_LD(ks + 1, j);
        acc[j] = mfma_bf(wf[ks & 1][j], af[ks], acc[j]);
        SCHED_FENCE();
      }
    }
    rx[0] = *(const u32x4*)(sm + 86016 + ((w & 3) * 4 + 0) * 1024 + vl);
    rx[1] = *(const u32x4*)(sm + 86016 + ((w & 3) * 4 + 1) * 1024 + vl);
    rh[0] = *(const u32x4*)(sm + 86016 + ((w & 3) * 4 + 2) * 1024 + vl);
    rh[1] = *(const u32x4*)(sm + 86016 + ((w & 3) * 4 + 3) * 1024 + vl);
    asm volatile("s_waitcnt vmcnt(0) lgkmcnt(0)\n\ts_barrier"
                 : "+v"(pa), "+v"(pb), "+v"(rx[0]), "+v"(rx[1]), "+v"(rh[0]), "+v"(rh[1]), "+v"(r4), "+v"(cs[0]), "+v"(cs[1]),
                   "+v"(cs[2]), "+v"(cs[3]), "+v"(cs[4]), "+v"(cs[5]), "+v"(cs[6])::"memory");
    const float s1 = sum_rows(pa.x + pb.x), s2 = sum_rows(pa.y + pb.y);
    const float mean = s1 * (1.0f / DM);
    const float var = fmaxf(s2 * (1.0f / DM) - mean * mean, 0.f);
    float mb, sb;
    ln_scalars(mean, var, mb, sb);
    const float is = __builtin_amdgcn_rcpf(sb);
    const float c1 = GC1 * is, c2 = GC2 * is * is * is;
    char* ao = ws + O_A3 + (size_t)u16t * 54 * 1024 + vl;
    auto act = [&](int j, int e) -> unsigned {
      return gelu2(fma2(bc2(-mb), f32x2{cs[j][e], cs[j][e + 1]}, f32x2{acc[j][e], acc[j][e + 1]}), c1, c2, sb);
    };
#pragma unroll
    for (int pr = 0; pr < 3; ++pr)
      st16(ao + (K0p + pr) * 1024, u32x4{act(2 * pr, 0), act(2 * pr, 2), act(2 * pr + 1, 0), act(2 * pr + 1, 2)});
    st8(ao + K6p * 1024 + ((2 * h6 + (g >> 1)) - g) * 256 + (g & 1) * 8, u32x2{act(6, 0), act(6, 2)});
    asm volatile("s_waitcnt vmcnt(0)" ::: "memory");
    if (lane == 0) signal_count(cnt, 256 + u16t);
  }
  if (w >= 4) {
    BAR();
    BAR(); BAR(); BAR(); BAR(); BAR(); BAR(); BAR(); BAR();
    return;
  }

  {
    const char* asrc = ws + O_A3 + (size_t)t16 * 54 * 1024;
    u32x4 af[54], wf[2][5];
    f32x4 acc[5];
#define S4_LD(ks, j) LDW(W4SLOT((ks) / 6) + ((j) * 6 + (ks) % 6) * 1024)
#define S4_A(k) GLD16S(af[k], asrc + ((k) >> 2) * 4096, vl, ((k) & 3) * 1024)
    wait_count(cnt, 256 + t16, lane == 0, 16u);
#pragma unroll
    for (int k = 0; k < 16; ++k) S4_A(k);
    BAR();
#pragma unroll
    for (int j = 0; j < 5; ++j) { acc[j] = f32x4{0.f, 0.f, 0.f, 0.f}; wf[0][j] = S4_LD(0, j); }
#pragma unroll
    for (int c = 0; c < 9; ++c)
#pragma unroll
    for (int k6 = 0; k6 < 6; ++k6) {
      const int ks = c * 6 + k6;
      if (k6 == 5 && c < 8) BAR();
      asm volatile("s_waitcnt vmcnt(%1)" : "+v"(af[ks]) : "i"(ks <= 38 ? 15 : 53 - ks) : "memory");
#pragma unroll
      for (int j = 0; j < 5; ++j) {
        if (j == 1 && ks < 38) S4_A(ks + 16);
        if (ks < 53) wf[(ks + 1) & 1][j] = S4_LD(ks + 1, j);
        acc[j] = mfma_bf(wf[ks & 1][j], af[ks], acc[j]);
        SCHED_FENCE();
      }
    }
    auto fin = [&](unsigned h2, unsigned x2, float a0, float a1) -> unsigned {
      unsigned hs = pkbf(a0, a1);
      unsigned bo = pkbf2(bup(x2) + bup(hs));
      return pkbf2(bup(h2) + bup(bo));
    };
    char* orow = (char*)out + (size_t)(t16 * 16 + i) * (DM * 2) + g * 16;
#pragma unroll
    for (int pr = 0; pr < 2; ++pr) {
      u32x4 o = {fin(rh[pr][0], rx[pr][0], acc[2 * pr][0], acc[2 * pr][1]), fin(rh[pr][1], rx[pr][1], acc[2 * pr][2], acc[2 * pr][3]),
                 fin(rh[pr][2], rx[pr][2], acc[2 * pr + 1][0], acc[2 * pr + 1][1]),
                 fin(rh[pr][3], rx[pr][3], acc[2 * pr + 1][2], acc[2 * pr + 1][3])};
      st16(orow + (J0 + pr) * 64, o);
    }
    st8(orow + J4 * 64 + h4 * 32 - g * 8, u32x2{fin(r4[2], r4[0], acc[4][0], acc[4][1]), fin(r4[3], r4[1], acc[4][2], acc[4][3])});
  }
}

__device__ __attribute__((noinline)) void text_end_marker();
__global__ __launch_bounds__(256, 4) void k_front(const u16* __restrict__ X, const u16* __restrict__ lAr,
                                                  const u16* __restrict__ Aim, const u16* __restrict__ Cre,
                                                  const u16* __restrict__ Cim, const u16* __restrict__ ldt,
                                                  const u16* __restrict__ Dsk, const u16* __restrict__ w1,
                                                  const u16* __restrict__ w2, const u16* __restrict__ w3,
                                                  const u16* __restrict__ w4, char* __restrict__ ws) {
  __shared__ __attribute__((aligned(16))) char sm[40960];
  const int b = blockIdx.x;
  if (b >= 256) {
    unsigned long long a0;
    asm volatile("s_getpc_b64 s[30:31]\n\ts_add_u32 s30, s30, _Z7k_chainPcPt@rel32@lo+4\n\t"
                 "s_addc_u32 s31, s31, _Z7k_chainPcPt@rel32@hi+12\n\ts_mov_b64 %0, s[30:31]"
                 : "=s"(a0)::"s30", "s31", "scc");
    role_chan(b - 256, lAr, Aim, Cre, Cim, ldt, Dsk, ws, sm, (const char*)a0, (const char*)(void*)&text_end_marker);
  }
  else if (b < 128) role_ln(b, X, ws, sm);
  else if (b == 128) { if (threadIdx.x < 64) role_collect(ws); }
  else if (b < 129 + 93) {
    const int pb = b - 129;
    __builtin_amdgcn_s_sleep(32);
    if (pb < 12) st16(ws + O_CNT + pb * 4096 + threadIdx.x * 16, u32x4{0, 0, 0, 0});
    role_pack(2 * pb, w1, w2, w3, w4, ws, sm);
    if (2 * pb + 1 < 185) role_pack(2 * pb + 1, w1, w2, w3, w4, ws, sm);
  } else {
    __builtin_amdgcn_s_sleep(32);
    role_colsum(b - 222, w1, w3, ws, sm);
    if (b - 222 + 34 < 37) { __syncthreads(); role_colsum(b - 222 + 34, w1, w3, ws, sm); }
  }
}

__device__ __attribute__((noinline)) void text_end_marker() { asm volatile("s_nop 0"); }

extern "C" void kernel_launch(void* const* d_in, const int* in_sizes, int n_in, void* d_out, int out_size, void* d_ws,
                              size_t ws_size, hipStream_t stream) {
  (void)in_sizes; (void)n_in; (void)out_size; (void)ws_size;
  const u16* X = (const u16*)d_in[0];
  char* ws = (char*)d_ws;
  k_front<<<1024, 256, 0, stream>>>(X, (const u16*)d_in[1], (const u16*)d_in[2], (const u16*)d_in[3], (const u16*)d_in[4],
                                    (const u16*)d_in[5], (const u16*)d_in[6], (const u16*)d_in[8], (const u16*)d_in[9],
                                    (const u16*)d_in[10], (const u16*)d_in[11], ws);
  k_chain<<<256, 768, 0, stream>>>(ws, (u16*)d_out);
}
```
